```python
import torch

def _softmax(x, axis=-1):
    return torch.softmax(x, dim=axis)
def _gelu(x):
    return torch.nn.functional.gelu(x, approximate="tanh")
def _silu(x):
    return torch.nn.functional.silu(x)
def _sigmoid(x):
    return torch.sigmoid(x)
def _layernorm(x, axis=-1, eps=1e-5):
    m = x.mean(dim=axis, keepdim=True)
    v = x.var(dim=axis, keepdim=True, unbiased=False)
    return (x - m) / torch.sqrt(v + eps)
def _scatter_add(tgt, idx, src, axis=0):
    return tgt.clone().index_add_(axis, idx, src)
def _segment_sum(src, seg, n):
    out = torch.zeros((n,) + src.shape[1:], dtype=src.dtype)
    return out.index_add_(0, seg, src)
def _pad(x, pw):
    flat = tuple(v for pair in reversed(pw) for v in pair)
    return torch.nn.functional.pad(x, flat)

kernel_name = 'standalone__deep_cross_wide_deep__recsys'

def setup_inputs():

    _w = (lambda a: (lambda t: t.to(torch.bfloat16) if t.is_floating_point() else t)(torch.as_tensor(a)))
    B, F, D = 4096, 26, 128
    V, Nd = min(100000, 100000), 13
    Din = F * D
    H = 4 * D
    return {
        "idx": _w((lambda lo, hi, *s: torch.randint(lo, hi, s))(0, V, B, F).to(torch.int64)),
        "table": _w((lambda *s: torch.randn(*s, dtype=torch.bfloat16) * 1.6)(V, D) * 0.05),
        "x_wide": _w((lambda *s: torch.randn(*s, dtype=torch.bfloat16) * 0.8)(B, Nd)),
        "w_wide": _w((lambda *s: torch.randn(*s, dtype=torch.bfloat16) * 0.8)(Nd) * 0.05),
        "b_wide": _w((lambda *s: torch.randn(*s, dtype=torch.bfloat16) * 0.8)(1) * 0.01),
        "W1": _w((lambda *s: torch.randn(*s, dtype=torch.bfloat16) * 1.6)(Din, H) * (2.0 / Din) ** 0.5),
        "b1": _w((lambda *s: torch.randn(*s, dtype=torch.bfloat16) * 1.6)(H) * 0.01),
        "W2": _w((lambda *s: torch.randn(*s, dtype=torch.bfloat16) * 1.6)(H) * (1.0 / H) ** 0.5),
        "b2": _w((lambda *s: torch.randn(*s, dtype=torch.bfloat16) * 1.6)(1) * 0.01),
    }

def reference(idx, table, x_wide, w_wide, b_wide, W1, b1, W2, b2):
    emb = table[idx]
    deep_in = emb.reshape(emb.shape[0], -1)
    h = deep_in @ W1 + b1
    h = h * (lambda x, dt: x.to(dt))(h > 0, h.dtype)
    deep_logit = h @ W2 + b2
    wide_logit = x_wide @ w_wide + b_wide
    return _sigmoid(wide_logit + deep_logit)
```

```cpp
#include <hip/hip_runtime.h>
#include <hip/hip_bf16.h>
#include <stdint.h>

typedef __attribute__((ext_vector_type(8))) short bf16x8;
typedef __attribute__((ext_vector_type(4))) short bf16x4;
typedef __attribute__((ext_vector_type(16))) float f32x16;
typedef __attribute__((ext_vector_type(4))) unsigned u32x4;

constexpr int Bsz = 4096, F = 26, D = 128, H = 512, ND = 13;

__device__ __forceinline__ float bf2f(unsigned short x) { return __uint_as_float(((unsigned)x) << 16); }
__device__ __forceinline__ float rbf(float x) { return __bfloat162float(__float2bfloat16(x)); }

__global__ __launch_bounds__(256) void k1(const int* __restrict__ idx, const unsigned short* __restrict__ table,
                                          const unsigned short* __restrict__ W1, const unsigned short* __restrict__ b1,
                                          const unsigned short* __restrict__ W2, float* __restrict__ part, int V) {
  __shared__ __attribute__((aligned(16))) unsigned short sB[64][128 + 8];
  const int tid = threadIdx.x, lane = tid & 63, w = tid >> 6;
  const int bid = blockIdx.x;
  const int xcd = bid & 7, slot = bid >> 3;
  const int mt = xcd * 4 + (slot >> 3), nt = slot & 7;
  const int m0 = mt * 128, n0 = nt * 64;
  const int r = lane & 31, h = lane >> 5;
  const int row = m0 + w * 32 + r;
  f32x16 acc0 = {}, acc1 = {};
  for (int f = 0; f < F; ++f) {
    __syncthreads();
    for (int e = tid; e < 128 * 64; e += 256) {
      int k = e >> 6, n = e & 63;
      sB[n][k] = W1[(size_t)(f * 128 + k) * H + n0 + n];
    }
    __syncthreads();
    int ix = idx[row * F + f];
    if (ix < 0) ix += V;
    const unsigned short* arow = table + (size_t)ix * D;
    for (int s = 0; s < 8; ++s) {
      bf16x8 a = *(const bf16x8*)(arow + 16 * s + 8 * h);
      bf16x8 w0 = *(const bf16x8*)(&sB[r][16 * s + 8 * h]);
      bf16x8 w1 = *(const bf16x8*)(&sB[32 + r][16 * s + 8 * h]);
      acc0 = __builtin_amdgcn_mfma_f32_32x32x16_bf16(w0, a, acc0, 0, 0, 0);
      acc1 = __builtin_amdgcn_mfma_f32_32x32x16_bf16(w1, a, acc1, 0, 0, 0);
    }
  }
  float sum = 0.f;
  for (int t = 0; t < 2; ++t) {
    for (int reg = 0; reg < 16; ++reg) {
      int n = n0 + 32 * t + (reg & 3) + 8 * (reg >> 2) + 4 * h;
      float v = t ? acc1[reg] : acc0[reg];
      v = rbf(v);
      v = rbf(v + bf2f(b1[n]));
      v = v > 0.f ? v : 0.f;
      sum += v * bf2f(W2[n]);
    }
  }
  sum += __shfl_xor(sum, 32);
  if (h == 0) part[nt * Bsz + row] = sum;
}

__global__ __launch_bounds__(256) void k2(const float* __restrict__ part, const unsigned short* __restrict__ xw,
                                          const unsigned short* __restrict__ ww, const unsigned short* __restrict__ bw,
                                          const unsigned short* __restrict__ b2, __hip_bfloat16* __restrict__ out) {
  int b = blockIdx.x * 256 + threadIdx.x;
  float s = 0.f;
  for (int j = 0; j < 8; ++j) s += part[j * Bsz + b];
  float deep = rbf(rbf(s) + bf2f(b2[0]));
  float wd = 0.f;
  for (int j = 0; j < ND; ++j) wd += bf2f(xw[b * ND + j]) * bf2f(ww[j]);
  float wide = rbf(rbf(wd) + bf2f(bw[0]));
  float z = rbf(wide + deep);
  out[b] = __float2bfloat16(1.f / (1.f + __expf(-z)));
}

extern "C" void kernel_launch(void* const* d_in, const int* in_sizes, int n_in, void* d_out, int out_size, void* d_ws,
                              size_t ws_size, hipStream_t stream) {
  const int* idx = (const int*)d_in[0];
  const unsigned short* table = (const unsigned short*)d_in[1];
  const unsigned short* xw = (const unsigned short*)d_in[2];
  const unsigned short* ww = (const unsigned short*)d_in[3];
  const unsigned short* bw = (const unsigned short*)d_in[4];
  const unsigned short* W1 = (const unsigned short*)d_in[5];
  const unsigned short* b1 = (const unsigned short*)d_in[6];
  const unsigned short* W2 = (const unsigned short*)d_in[7];
  const unsigned short* b2 = (const unsigned short*)d_in[8];
  int V = in_sizes[1] / D;
  float* part = (float*)d_ws;
  k1<<<256, 256, 0, stream>>>(idx, table, W1, b1, W2, part, V);
  k2<<<Bsz / 256, 256, 0, stream>>>(part, xw, ww, bw, b2, (__hip_bfloat16*)d_out);
}
```

```cpp
#include <hip/hip_runtime.h>
#include <hip/hip_bf16.h>
#include <stdint.h>

typedef __attribute__((ext_vector_type(8))) short bf16x8;
typedef __attribute__((ext_vector_type(4))) short bf16x4;
typedef __attribute__((ext_vector_type(16))) float f32x16;
typedef __attribute__((ext_vector_type(4))) unsigned u32x4;

constexpr int Bsz = 4096, F = 26, D = 128, H = 512, ND = 13;

__device__ __forceinline__ float bf2f(unsigned short x) { return __uint_as_float(((unsigned)x) << 16); }
__device__ __forceinline__ float rbf(float x) { return __bfloat162float(__float2bfloat16(x)); }

__global__ __launch_bounds__(256) void k1(const int* __restrict__ idx, const unsigned short* __restrict__ table,
                                          const unsigned short* __restrict__ W1, const unsigned short* __restrict__ b1,
                                          const unsigned short* __restrict__ W2, float* __restrict__ part, int V) {
  __shared__ __attribute__((aligned(16))) unsigned short sB[64][128 + 8];
  const int tid = threadIdx.x, lane = tid & 63, w = tid >> 6;
  const int bid = blockIdx.x;
  const int xcd = bid & 7, slot = bid >> 3;
  const int mt = xcd * 4 + (slot >> 3), nt = slot & 7;
  const int m0 = mt * 128, n0 = nt * 64;
  const int r = lane & 31, h = lane >> 5;
  const int row = m0 + w * 32 + r;
  f32x16 acc0 = {}, acc1 = {};
  for (int f = 0; f < F; ++f) {
    __syncthreads();
    for (int e = tid; e < 128 * 64; e += 256) {
      int k = e >> 6, n = e & 63;
      sB[n][k] = W1[(size_t)(f * 128 + k) * H + n0 + n];
    }
    __syncthreads();
    int ix = idx[row * F + f];
    if (ix < 0) ix += V;
    const unsigned short* arow = table + (size_t)ix * D;
    for (int s = 0; s < 8; ++s) {
      bf16x8 a = *(const bf16x8*)(arow + 16 * s + 8 * h);
      bf16x8 w0 = *(const bf16x8*)(&sB[r][16 * s + 8 * h]);
      bf16x8 w1 = *(const bf16x8*)(&sB[32 + r][16 * s + 8 * h]);
      acc0 = __builtin_amdgcn_mfma_f32_32x32x16_bf16(w0, a, acc0, 0, 0, 0);
      acc1 = __builtin_amdgcn_mfma_f32_32x32x16_bf16(w1, a, acc1, 0, 0, 0);
    }
  }
  float sum = 0.f;
  for (int t = 0; t < 2; ++t) {
    for (int reg = 0; reg < 16; ++reg) {
      int n = n0 + 32 * t + (reg & 3) + 8 * (reg >> 2) + 4 * h;
      float v = t ? acc1[reg] : acc0[reg];
      v = rbf(v);
      v = rbf(v + bf2f(b1[n]));
      v = v > 0.f ? v : 0.f;
      sum += v * bf2f(W2[n]);
    }
  }
  sum += __shfl_xor(sum, 32);
  if (h == 0) part[nt * Bsz + row] = sum;
}

__global__ __launch_bounds__(256) void k2(const float* __restrict__ part, const unsigned short* __restrict__ xw,
                                          const unsigned short* __restrict__ ww, const unsigned short* __restrict__ bw,
                                          const unsigned short* __restrict__ b2, __hip_bfloat16* __restrict__ out) {
  int b = blockIdx.x * 256 + threadIdx.x;
  float s = 0.f;
  for (int j = 0; j < 8; ++j) s += part[j * Bsz + b];
  float deep = rbf(rbf(s) + bf2f(b2[0]));
  float wd = 0.f;
  for (int j = 0; j < ND; ++j) wd += bf2f(xw[b * ND + j]) * bf2f(ww[j]);
  float wide = rbf(rbf(wd) + bf2f(bw[0]));
  float z = rbf(wide + deep);
  out[b] = __float2bfloat16(1.f / (1.f + __expf(-z)));
}


typedef __attribute__((ext_vector_type(4))) int i32x4;
__device__ __forceinline__ i32x4 mk_rsrc(const void* p, unsigned bytes) {
  unsigned long long a = (unsigned long long)p;
  i32x4 r; r[0] = __builtin_amdgcn_readfirstlane((int)(unsigned)a); r[1] = __builtin_amdgcn_readfirstlane((int)((unsigned)(a >> 32) & 0xffff));
  r[2] = __builtin_amdgcn_readfirstlane((int)bytes); r[3] = 0x00020000; return r;
}
__device__ __forceinline__ void bdma(unsigned ldsbase, unsigned voff, i32x4 rs, unsigned soff) {
  asm volatile("s_mov_b32 m0, %0\n\ts_nop 0\n\tbuffer_load_dwordx4 %1, %2, %3 offen lds" :: "s"(ldsbase), "v"(voff), "s"(rs), "s"(soff) : "memory");
}
__global__ __launch_bounds__(256) void probe(const unsigned short* __restrict__ table, unsigned long long* __restrict__ stamps) {
  __shared__ __attribute__((aligned(16))) char smem[49152];
  unsigned long long t0 = __builtin_amdgcn_s_memrealtime();
  const int tid = threadIdx.x, lane = tid & 63;
  const int w = __builtin_amdgcn_readfirstlane(tid >> 6);
  const int bid = blockIdx.x;
  i32x4 rsT = mk_rsrc(table, 100000 * 128 * 2);
  const unsigned lds0 = (unsigned)(uintptr_t)smem;
  unsigned vo = (unsigned)(bid * 98304 + w * 1024 + lane * 16);
#pragma unroll
  for (int i = 0; i < 24; ++i) bdma(lds0 + (unsigned)((i % 12) * 4096 + w * 1024), vo, rsT, (unsigned)(i * 4096));
  asm volatile("s_waitcnt vmcnt(0)" ::: "memory");
  __syncthreads();
  unsigned long long t1 = __builtin_amdgcn_s_memrealtime();
  if (tid == 0) { stamps[2 * bid] = t0; stamps[2 * bid + 1] = t1; }
}
__global__ void report(const unsigned long long* __restrict__ stamps, const void* a, const void* b, const void* c, const void* d) {
  unsigned long long mn = ~0ull, mx = 0, mxs = 0; double ad = 0;
  for (int i = 0; i < 256; ++i) { mn = stamps[2*i] < mn ? stamps[2*i] : mn; mxs = stamps[2*i] > mxs ? stamps[2*i] : mxs; mx = stamps[2*i+1] > mx ? stamps[2*i+1] : mx; ad += (double)(stamps[2*i+1] - stamps[2*i]); }
  printf("PROBE span %.2f avg %.2f startskew %.2f  idx %p table %p W1 %p ws %p\n", (mx - mn) * 0.01, ad / 256 * 0.01, (mxs - mn) * 0.01, a, b, c, d);
}

extern "C" void kernel_launch(void* const* d_in, const int* in_sizes, int n_in, void* d_out, int out_size, void* d_ws,
                              size_t ws_size, hipStream_t stream) {
  const int* idx = (const int*)d_in[0];
  const unsigned short* table = (const unsigned short*)d_in[1];
  const unsigned short* xw = (const unsigned short*)d_in[2];
  const unsigned short* ww = (const unsigned short*)d_in[3];
  const unsigned short* bw = (const unsigned short*)d_in[4];
  const unsigned short* W1 = (const unsigned short*)d_in[5];
  const unsigned short* b1 = (const unsigned short*)d_in[6];
  const unsigned short* W2 = (const unsigned short*)d_in[7];
  const unsigned short* b2 = (const unsigned short*)d_in[8];
  int V = in_sizes[1] / D;
  float* part = (float*)d_ws;
  unsigned long long* st = (unsigned long long*)((char*)d_ws + (1 << 20));
  probe<<<256, 256, 0, stream>>>(table, st);
  report<<<1, 1, 0, stream>>>(st, idx, table, W1, d_ws);
  k1<<<256, 256, 0, stream>>>(idx, table, W1, b1, W2, part, V);
  k2<<<Bsz / 256, 256, 0, stream>>>(part, xw, ww, bw, b2, (__hip_bfloat16*)d_out);
}
```

```cpp
#define AMP 0
#include <hip/hip_runtime.h>
#include <hip/hip_bf16.h>
#include <stdint.h>

typedef __attribute__((ext_vector_type(8))) short bf16x8;
typedef __attribute__((ext_vector_type(4))) short bf16x4;
typedef __attribute__((ext_vector_type(16))) float f32x16;
typedef __attribute__((ext_vector_type(4))) unsigned u32x4;

constexpr int Bsz = 4096, F = 26, D = 128, H = 512, ND = 13;

__device__ __forceinline__ float bf2f(unsigned short x) { return __uint_as_float(((unsigned)x) << 16); }
__device__ __forceinline__ float rbf(float x) { return __bfloat162float(__float2bfloat16(x)); }

__global__ __launch_bounds__(256) void k1(const int* __restrict__ idx, const unsigned short* __restrict__ table,
                                          const unsigned short* __restrict__ W1, const unsigned short* __restrict__ b1,
                                          const unsigned short* __restrict__ W2, float* __restrict__ part, int V) {
  __shared__ __attribute__((aligned(16))) unsigned short sB[64][128 + 8];
  const int tid = threadIdx.x, lane = tid & 63, w = tid >> 6;
  const int bid = blockIdx.x;
  const int xcd = bid & 7, slot = bid >> 3;
  const int mt = xcd * 4 + (slot >> 3), nt = slot & 7;
  const int m0 = mt * 128, n0 = nt * 64;
  const int r = lane & 31, h = lane >> 5;
  const int row = m0 + w * 32 + r;
  f32x16 acc0 = {}, acc1 = {};
  for (int f = 0; f < F; ++f) {
    __syncthreads();
    for (int e = tid; e < 128 * 64; e += 256) {
      int k = e >> 6, n = e & 63;
      sB[n][k] = W1[(size_t)(f * 128 + k) * H + n0 + n];
    }
    __syncthreads();
    int ix = idx[row * F + f];
    if (ix < 0) ix += V;
    const unsigned short* arow = table + (size_t)ix * D;
    for (int s = 0; s < 8; ++s) {
      bf16x8 a = *(const bf16x8*)(arow + 16 * s + 8 * h);
      bf16x8 w0 = *(const bf16x8*)(&sB[r][16 * s + 8 * h]);
      bf16x8 w1 = *(const bf16x8*)(&sB[32 + r][16 * s + 8 * h]);
      acc0 = __builtin_amdgcn_mfma_f32_32x32x16_bf16(w0, a, acc0, 0, 0, 0);
      acc1 = __builtin_amdgcn_mfma_f32_32x32x16_bf16(w1, a, acc1, 0, 0, 0);
    }
  }
  float sum = 0.f;
  for (int t = 0; t < 2; ++t) {
    for (int reg = 0; reg < 16; ++reg) {
      int n = n0 + 32 * t + (reg & 3) + 8 * (reg >> 2) + 4 * h;
      float v = t ? acc1[reg] : acc0[reg];
      v = rbf(v);
      v = rbf(v + bf2f(b1[n]));
      v = v > 0.f ? v : 0.f;
      sum += v * bf2f(W2[n]);
    }
  }
  sum += __shfl_xor(sum, 32);
  if (h == 0) part[nt * Bsz + row] = sum;
}

__global__ __launch_bounds__(256) void k2(const float* __restrict__ part, const unsigned short* __restrict__ xw,
                                          const unsigned short* __restrict__ ww, const unsigned short* __restrict__ bw,
                                          const unsigned short* __restrict__ b2, __hip_bfloat16* __restrict__ out) {
  int b = blockIdx.x * 256 + threadIdx.x;
  float s = 0.f;
  for (int j = 0; j < 8; ++j) s += part[j * Bsz + b];
  float deep = rbf(rbf(s) + bf2f(b2[0]));
  float wd = 0.f;
  for (int j = 0; j < ND; ++j) wd += bf2f(xw[b * ND + j]) * bf2f(ww[j]);
  float wide = rbf(rbf(wd) + bf2f(bw[0]));
  float z = rbf(wide + deep);
  out[b] = __float2bfloat16(1.f / (1.f + __expf(-z)));
}


typedef __attribute__((ext_vector_type(4))) int i32x4;
__device__ __forceinline__ i32x4 mk_rsrc(const void* p, unsigned bytes) {
  unsigned long long a = (unsigned long long)p;
  i32x4 r; r[0] = __builtin_amdgcn_readfirstlane((int)(unsigned)a); r[1] = __builtin_amdgcn_readfirstlane((int)((unsigned)(a >> 32) & 0xffff));
  r[2] = __builtin_amdgcn_readfirstlane((int)bytes); r[3] = 0x00020000; return r;
}
__device__ __forceinline__ void bdma(unsigned ldsbase, unsigned voff, i32x4 rs, unsigned soff) {
  asm volatile("s_mov_b32 m0, %0\n\ts_nop 0\n\tbuffer_load_dwordx4 %1, %2, %3 offen lds" :: "s"(ldsbase), "v"(voff), "s"(rs), "s"(soff) : "memory");
}
__global__ __launch_bounds__(256) void probe(const unsigned short* __restrict__ table, unsigned long long* __restrict__ stamps) {
  __shared__ __attribute__((aligned(16))) char smem[49152];
  unsigned long long t0 = __builtin_amdgcn_s_memrealtime();
  const int tid = threadIdx.x, lane = tid & 63;
  const int w = __builtin_amdgcn_readfirstlane(tid >> 6);
  const int bid = blockIdx.x;
  i32x4 rsT = mk_rsrc(table, 100000 * 128 * 2);
  const unsigned lds0 = (unsigned)(uintptr_t)smem;
  unsigned vo = (unsigned)(bid * 98304 + w * 1024 + lane * 16);
#pragma unroll
  for (int i = 0; i < 24; ++i) bdma(lds0 + (unsigned)((i % 12) * 4096 + w * 1024), vo, rsT, (unsigned)(i * 4096));
  asm volatile("s_waitcnt vmcnt(0)" ::: "memory");
  __syncthreads();
  unsigned long long t1 = __builtin_amdgcn_s_memrealtime();
  if (tid == 0) { stamps[2 * bid] = t0; stamps[2 * bid + 1] = t1; }
}
__global__ void report(const unsigned long long* __restrict__ stamps, const void* a, const void* b, const void* c, const void* d) {
  unsigned long long mn = ~0ull, mx = 0;
  for (int i = 0; i < 256; ++i) { mn = stamps[2*i] < mn ? stamps[2*i] : mn; mx = stamps[2*i+1] > mx ? stamps[2*i+1] : mx; }
  unsigned long long span = mx - mn;
  if (span > 5000) span = 5000;
  unsigned long long t0 = __builtin_amdgcn_s_memrealtime();
  while (__builtin_amdgcn_s_memrealtime() - t0 < span * AMP) __builtin_amdgcn_s_sleep(1);
}

extern "C" void kernel_launch(void* const* d_in, const int* in_sizes, int n_in, void* d_out, int out_size, void* d_ws,
                              size_t ws_size, hipStream_t stream) {
  const int* idx = (const int*)d_in[0];
  const unsigned short* table = (const unsigned short*)d_in[1];
  const unsigned short* xw = (const unsigned short*)d_in[2];
  const unsigned short* ww = (const unsigned short*)d_in[3];
  const unsigned short* bw = (const unsigned short*)d_in[4];
  const unsigned short* W1 = (const unsigned short*)d_in[5];
  const unsigned short* b1 = (const unsigned short*)d_in[6];
  const unsigned short* W2 = (const unsigned short*)d_in[7];
  const unsigned short* b2 = (const unsigned short*)d_in[8];
  int V = in_sizes[1] / D;
  float* part = (float*)d_ws;
  unsigned long long* st = (unsigned long long*)((char*)d_ws + (1 << 20));
  probe<<<256, 256, 0, stream>>>(table, st);
  report<<<1, 1, 0, stream>>>(st, idx, table, W1, d_ws);
  k1<<<256, 256, 0, stream>>>(idx, table, W1, b1, W2, part, V);
  k2<<<Bsz / 256, 256, 0, stream>>>(part, xw, ww, bw, b2, (__hip_bfloat16*)d_out);
}
```

```cpp
#include <hip/hip_runtime.h>
#include <hip/hip_bf16.h>
#include <stdint.h>

typedef __attribute__((ext_vector_type(8))) short bf16x8;
typedef __attribute__((ext_vector_type(4))) short bf16x4;
typedef __attribute__((ext_vector_type(16))) float f32x16;
typedef __attribute__((ext_vector_type(4))) float f32x4;
typedef __attribute__((ext_vector_type(4))) unsigned u32x4;
typedef __attribute__((ext_vector_type(2))) unsigned u32x2;
typedef __attribute__((ext_vector_type(4))) int i32x4;

constexpr int Bsz = 4096, F = 26, D = 128, H = 512, ND = 13;
constexpr int SLOT = 49152, NSLOT = 3, IDXOFF = SLOT * NSLOT, LDS_BYTES = IDXOFF + 13312 + 768;

__device__ __forceinline__ float bf2f(unsigned short x) { return __uint_as_float(((unsigned)x) << 16); }
__device__ __forceinline__ float rbf(float x) { return __bfloat162float(__float2bfloat16(x)); }

__device__ __forceinline__ i32x4 mk_rsrc(const void* p, unsigned bytes) {
  unsigned long long a = (unsigned long long)p;
  i32x4 r;
  r[0] = __builtin_amdgcn_readfirstlane((int)(unsigned)a);
  r[1] = __builtin_amdgcn_readfirstlane((int)((unsigned)(a >> 32) & 0xffff));
  r[2] = __builtin_amdgcn_readfirstlane((int)bytes);
  r[3] = 0x00020000;
  return r;
}
__device__ __forceinline__ void bdma(unsigned ldsbase, unsigned voff, i32x4 rs, unsigned soff) {
  asm volatile("s_mov_b32 m0, %0\n\ts_nop 0\n\tbuffer_load_dwordx4 %1, %2, %3 offen lds" ::"s"(ldsbase), "v"(voff), "s"(rs), "s"(soff) : "memory");
}
template <int N> __device__ __forceinline__ void waitv() { asm volatile("s_waitcnt vmcnt(%0)" ::"i"(N) : "memory"); }
__device__ __forceinline__ void bar() { asm volatile("s_barrier" ::: "memory"); }
__device__ __forceinline__ void bar_lgkm() { asm volatile("s_waitcnt lgkmcnt(0)\n\ts_barrier" ::: "memory"); }

typedef __attribute__((address_space(3))) char lds_char;
typedef __attribute__((address_space(3))) bf16x8 lds_bf16x8;
typedef __attribute__((address_space(3))) bf16x4 lds_bf16x4;
typedef __attribute__((address_space(3))) int lds_int;
typedef __attribute__((address_space(3))) f32x4 lds_f32x4;

__device__ __forceinline__ int field_of(int s, int r, int rot) {
  int f;
  if (s == 0) f = r;
  else {
    int tstar = 22 + ((r + 2) & 3);
    int t = s - 1;
    t += (t >= tstar) ? 1 : 0;
    f = ((t & 3) == r) ? t + 4 : t;
  }
  f += rot;
  return f >= F ? f - F : f;
}

__global__ __launch_bounds__(512) void k1(const int* __restrict__ idx, const unsigned short* __restrict__ table,
                                          const unsigned short* __restrict__ W1, const unsigned short* __restrict__ b1,
                                          const unsigned short* __restrict__ W2, float* __restrict__ part, int V) {
  extern __shared__ __attribute__((aligned(16))) char smem[];
  lds_char* lds = (lds_char*)smem;
  const unsigned lds0 = (unsigned)(uintptr_t)lds;
  const int tid = threadIdx.x, lane = tid & 63;
  const int w = __builtin_amdgcn_readfirstlane(tid >> 6);
  const int bid = blockIdx.x;
  const int xcd = bid & 7, slot = bid >> 3;
  const int ml = slot >> 3, nt = slot & 7;
  const int mt = xcd * 4 + ml;
  const int m0 = mt * 128, n0 = nt * 64;

  if (w >= 4) {
    const int lw = w - 4;
    const int g = lane >> 3, c = lane & 7;
    i32x4 rsI = mk_rsrc(idx, Bsz * F * 4);
    i32x4 rsT = mk_rsrc(table, (unsigned)V * D * 2);
    i32x4 rsW = mk_rsrc(W1, F * D * H * 2);
    asm volatile("s_nop 4");
    {
      unsigned vo = (unsigned)(lane * 16);
      unsigned so = (unsigned)((m0 + lw * 32) * F * 4);
#pragma unroll
      for (int j = 0; j < 4; ++j) bdma(lds0 + IDXOFF + lw * 3328 + j * 1024, vo, rsI, so + j * 1024);
    }
    const int r = (ml - nt) & 3, rot = 3 * xcd;
    const unsigned boff = (unsigned)(((lw * 32 + g) * H + n0) * 2 + ((c ^ (((g >> 1) & 1) << 2)) << 4));
    const unsigned sw0 = (unsigned)((c ^ (g >> 1)) << 4), sw1 = (unsigned)((c ^ (4 + (g >> 1))) << 4);
    const unsigned h0 = (unsigned)((nt >> 2) * 128), h1 = 128u - h0;
    auto issueB = [&](int f, unsigned sb) {
#pragma unroll
      for (int j = 0; j < 4; ++j) bdma(sb + lw * 4096 + j * 1024, boff, rsW, (unsigned)((f * 128 + j * 8) * H * 2));
    };
    const lds_int* sidx = (const lds_int*)(lds + IDXOFF) + (lw * 32 + g) * F;
    auto getoff = [&](int f, unsigned (&vo)[4]) {
#pragma unroll
      for (int j = 0; j < 4; ++j) {
        int ix = sidx[j * 8 * F + f];
        ix += (ix >> 31) & V;
        vo[j] = ((unsigned)ix << 8) | ((j & 1) ? sw1 : sw0);
      }
    };
    auto issueA = [&](unsigned (&vo)[4], unsigned sb) {
#pragma unroll
      for (int j = 0; j < 4; ++j) bdma(sb + 16384 + h0 * 128 + lw * 4096 + j * 1024, vo[j], rsT, h0);
#pragma unroll
      for (int j = 0; j < 4; ++j) bdma(sb + 16384 + h1 * 128 + lw * 4096 + j * 1024, vo[j], rsT, h1);
    };
    const int f0 = field_of(0, r, rot), f1 = field_of(1, r, rot), f2 = field_of(2, r, rot);
    issueB(f0, lds0); issueB(f1, lds0 + SLOT); issueB(f2, lds0 + 2 * SLOT);
    waitv<12>();
    unsigned vo[4];
    getoff(f0, vo); issueA(vo, lds0);
    getoff(f1, vo); issueA(vo, lds0 + SLOT);
    getoff(f2, vo); issueA(vo, lds0 + 2 * SLOT);
    int fn = field_of(3, r, rot);
    getoff(fn, vo);
    waitv<16>(); bar();
    waitv<8>();
    unsigned sb = lds0;
    for (int s = 1; s < F - 2; ++s) {
      bar();
      issueB(fn, sb); issueA(vo, sb);
      sb += SLOT; if (sb == lds0 + NSLOT * SLOT) sb = lds0;
      fn = field_of(s + 3 < F ? s + 3 : F - 1, r, rot);
      getoff(fn, vo);
      waitv<12>();
    }
    bar();
    waitv<0>(); bar();
    bar();
    bar();
  } else {
    const int kw = w & 1, mw = w >> 1;
    const int h = lane >> 5, r32 = lane & 31;
    u32x2 pb[8], pw[8];
#pragma unroll
    for (int i = 0; i < 8; ++i) {
      int n = n0 + 8 * i + 4 * h;
      pb[i] = *(const u32x2*)(b1 + n);
      pw[i] = *(const u32x2*)(W2 + n);
    }
    const int G = lane >> 4, q = (lane >> 2) & 3, p = lane & 3;
    unsigned ba[2], aa[4][2];
    {
      int krow = kw * 64 + 8 * (G >> 1) + q;
      int ch = 2 * (G & 1) + (p >> 1);
      ba[0] = (unsigned)(krow * 128 + ((ch + 4 * (q >> 1)) << 4) + 8 * (p & 1));
      ba[1] = (unsigned)(krow * 128 + ((ch + 4 * (1 - (q >> 1))) << 4) + 8 * (p & 1));
      int key = (r32 >> 1) & 7;
#pragma unroll
      for (int j = 0; j < 4; ++j) {
        unsigned a = (unsigned)(16384 + kw * 16384 + (mw * 64 + r32) * 128 + (((2 * j + h) ^ key) << 4));
        aa[j][0] = a + kw * 4096;
        aa[j][1] = a + (1 - kw) * 4096;
      }
    }
    f32x16 acc[2][2] = {};
    unsigned so = 0;
    for (int s = 0; s < F; ++s) {
      bar_lgkm();
      lds_char* sp = lds + so;
#pragma unroll
      for (int j = 0; j < 4; ++j) {
        bf16x8 wf[2], ef[2];
#pragma unroll
        for (int t = 0; t < 2; ++t) {
          bf16x4 lo = __builtin_amdgcn_ds_read_tr16_b64_v4i16((lds_bf16x4*)(sp + ba[t] + j * 2048));
          bf16x4 hi = __builtin_amdgcn_ds_read_tr16_b64_v4i16((lds_bf16x4*)(sp + ba[t] + j * 2048 + 512));
          wf[t] = __builtin_shufflevector(lo, hi, 0, 1, 2, 3, 4, 5, 6, 7);
        }
#pragma unroll
        for (int m = 0; m < 2; ++m) ef[m] = *(lds_bf16x8*)(sp + aa[j][m]);
#pragma unroll
        for (int t = 0; t < 2; ++t)
#pragma unroll
          for (int m = 0; m < 2; ++m) acc[t][m] = __builtin_amdgcn_mfma_f32_32x32x16_bf16(wf[t], ef[m], acc[t][m], 0, 0, 0);
      }
      so += SLOT; if (so == NSLOT * SLOT) so = 0;
    }
    bar_lgkm();
    {
      lds_f32x4* dst = (lds_f32x4*)(lds + w * 8192) + lane;
#pragma unroll
      for (int t = 0; t < 2; ++t)
#pragma unroll
        for (int i = 0; i < 4; ++i) {
          f32x16 a = acc[t][1];
          f32x4 v = {a[4 * i], a[4 * i + 1], a[4 * i + 2], a[4 * i + 3]};
          dst[(t * 4 + i) * 64] = v;
        }
    }
    bar_lgkm();
    float sum = 0.f;
    {
      const lds_f32x4* src = (const lds_f32x4*)(lds + (w ^ 1) * 8192) + lane;
#pragma unroll
      for (int t = 0; t < 2; ++t)
#pragma unroll
        for (int i = 0; i < 4; ++i) {
          f32x16 a = acc[t][0];
          f32x4 o = src[(t * 4 + i) * 64];
          u32x2 bb = pb[t * 4 + i], ww = pw[t * 4 + i];
#pragma unroll
          for (int e = 0; e < 4; ++e) {
            float v = rbf(a[4 * i + e] + o[e]);
            unsigned bw = bb[e >> 1], w2 = ww[e >> 1];
            float bv = __uint_as_float((e & 1) ? (bw & 0xffff0000u) : (bw << 16));
            float wv = __uint_as_float((e & 1) ? (w2 & 0xffff0000u) : (w2 << 16));
            v = rbf(v + bv);
            v = v > 0.f ? v : 0.f;
            sum += v * wv;
          }
        }
    }
    sum += __shfl_xor(sum, 32);
    if (h == 0) part[nt * Bsz + m0 + mw * 64 + kw * 32 + r32] = sum;
  }
}

__global__ __launch_bounds__(256) void k2(const float* __restrict__ part, const unsigned short* __restrict__ xw,
                                          const unsigned short* __restrict__ ww, const unsigned short* __restrict__ bw,
                                          const unsigned short* __restrict__ b2, __hip_bfloat16* __restrict__ out) {
  int b = blockIdx.x * 64 + threadIdx.x;
  float s = 0.f;
#pragma unroll
  for (int j = 0; j < 8; ++j) s += part[j * Bsz + b];
  float deep = rbf(rbf(s) + bf2f(b2[0]));
  float wd = 0.f;
#pragma unroll
  for (int j = 0; j < ND; ++j) wd += bf2f(xw[b * ND + j]) * bf2f(ww[j]);
  float wide = rbf(rbf(wd) + bf2f(bw[0]));
  float z = rbf(wide + deep);
  out[b] = __float2bfloat16(1.f / (1.f + __expf(-z)));
}

extern "C" void kernel_launch(void* const* d_in, const int* in_sizes, int n_in, void* d_out, int out_size, void* d_ws,
                              size_t ws_size, hipStream_t stream) {
  const int* idx = (const int*)d_in[0];
  const unsigned short* table = (const unsigned short*)d_in[1];
  const unsigned short* xw = (const unsigned short*)d_in[2];
  const unsigned short* ww = (const unsigned short*)d_in[3];
  const unsigned short* bw = (const unsigned short*)d_in[4];
  const unsigned short* W1 = (const unsigned short*)d_in[5];
  const unsigned short* b1 = (const unsigned short*)d_in[6];
  const unsigned short* W2 = (const unsigned short*)d_in[7];
  const unsigned short* b2 = (const unsigned short*)d_in[8];
  int V = in_sizes[1] / D;
  float* part = (float*)d_ws;
  hipFuncSetAttribute((const void*)k1, hipFuncAttributeMaxDynamicSharedMemorySize, LDS_BYTES);
  k1<<<256, 512, LDS_BYTES, stream>>>(idx, table, W1, b1, W2, part, V);
  k2<<<Bsz / 64, 64, 0, stream>>>(part, xw, ww, bw, b2, (__hip_bfloat16*)d_out);
}
```

```cpp
#include <hip/hip_runtime.h>
#include <hip/hip_bf16.h>
#include <stdint.h>

typedef __attribute__((ext_vector_type(8))) short bf16x8;
typedef __attribute__((ext_vector_type(4))) short bf16x4;
typedef __attribute__((ext_vector_type(16))) float f32x16;
typedef __attribute__((ext_vector_type(4))) float f32x4;
typedef __attribute__((ext_vector_type(4))) unsigned u32x4;
typedef __attribute__((ext_vector_type(2))) unsigned u32x2;
typedef __attribute__((ext_vector_type(4))) int i32x4;

constexpr int Bsz = 4096, F = 26, D = 128, H = 512, ND = 13;
constexpr int SLOT = 49152, NSLOT = 3, IDXOFF = SLOT * NSLOT, LDS_BYTES = IDXOFF + 13312 + 768;

__device__ __forceinline__ float bf2f(unsigned short x) { return __uint_as_float(((unsigned)x) << 16); }
__device__ __forceinline__ float rbf(float x) { return __bfloat162float(__float2bfloat16(x)); }

__device__ __forceinline__ i32x4 mk_rsrc(const void* p, unsigned bytes) {
  unsigned long long a = (unsigned long long)p;
  i32x4 r;
  r[0] = __builtin_amdgcn_readfirstlane((int)(unsigned)a);
  r[1] = __builtin_amdgcn_readfirstlane((int)((unsigned)(a >> 32) & 0xffff));
  r[2] = __builtin_amdgcn_readfirstlane((int)bytes);
  r[3] = 0x00020000;
  return r;
}
__device__ __forceinline__ void bdma(unsigned ldsbase, unsigned voff, i32x4 rs, unsigned soff) {
  asm volatile("s_mov_b32 m0, %0\n\ts_nop 0\n\tbuffer_load_dwordx4 %1, %2, %3 offen lds" ::"s"(ldsbase), "v"(voff), "s"(rs), "s"(soff) : "memory");
}
template <int N> __device__ __forceinline__ void waitv() { asm volatile("s_waitcnt vmcnt(%0)" ::"i"(N) : "memory"); }
__device__ __forceinline__ void bar() { asm volatile("s_barrier" ::: "memory"); }
__device__ __forceinline__ void bar_lgkm() { asm volatile("s_waitcnt lgkmcnt(0)\n\ts_barrier" ::: "memory"); }

typedef __attribute__((address_space(3))) char lds_char;
typedef __attribute__((address_space(3))) bf16x8 lds_bf16x8;
typedef __attribute__((address_space(3))) bf16x4 lds_bf16x4;
typedef __attribute__((address_space(3))) int lds_int;
typedef __attribute__((address_space(3))) f32x4 lds_f32x4;

__device__ __forceinline__ int field_of(int s, int r, int rot) {
  int f;
  if (s == 0) f = r;
  else {
    int tstar = 22 + ((r + 2) & 3);
    int t = s - 1;
    t += (t >= tstar) ? 1 : 0;
    f = ((t & 3) == r) ? t + 4 : t;
  }
  f += rot;
  return f >= F ? f - F : f;
}

__global__ __launch_bounds__(512, 2) void k1(const int* __restrict__ idx, const unsigned short* __restrict__ table,
                                          const unsigned short* __restrict__ W1, const unsigned short* __restrict__ b1,
                                          const unsigned short* __restrict__ W2, float* __restrict__ part, int V) {
  extern __shared__ __attribute__((aligned(16))) char smem[];
  lds_char* lds = (lds_char*)smem;
  const unsigned lds0 = (unsigned)(uintptr_t)lds;
  const int tid = threadIdx.x, lane = tid & 63;
  const int w = __builtin_amdgcn_readfirstlane(tid >> 6);
  const int bid = blockIdx.x;
  const int xcd = bid & 7, slot = bid >> 3;
  const int ml = slot >> 3, nt = slot & 7;
  const int mt = xcd * 4 + ml;
  const int m0 = mt * 128, n0 = nt * 64;

  if (w >= 4) {
    const int lw = w - 4;
    const int g = lane >> 3, c = lane & 7;
    i32x4 rsI = mk_rsrc(idx, Bsz * F * 4);
    i32x4 rsT = mk_rsrc(table, (unsigned)V * D * 2);
    i32x4 rsW = mk_rsrc(W1, F * D * H * 2);
    asm volatile("s_nop 4");
    {
      unsigned vo = (unsigned)(lane * 16);
      unsigned so = (unsigned)((m0 + lw * 32) * F * 4);
#pragma unroll
      for (int j = 0; j < 4; ++j) bdma(lds0 + IDXOFF + lw * 3328 + j * 1024, vo, rsI, so + j * 1024);
    }
    const int r = (ml - nt) & 3, rot = 3 * xcd;
    const unsigned boff = (unsigned)(((lw * 32 + g) * H + n0) * 2 + ((c ^ (((g >> 1) & 1) << 2)) << 4));
    const unsigned sw0 = (unsigned)((c ^ (g >> 1)) << 4), sw1 = (unsigned)((c ^ (4 + (g >> 1))) << 4);
    const unsigned h0 = (unsigned)((nt >> 2) * 128), h1 = 128u - h0;
    auto issueB = [&](int f, unsigned sb) {
#pragma unroll
      for (int j = 0; j < 4; ++j) bdma(sb + lw * 4096 + j * 1024, boff, rsW, (unsigned)((f * 128 + j * 8) * H * 2));
    };
    const lds_int* sidx = (const lds_int*)(lds + IDXOFF) + (lw * 32 + g) * F;
    auto getoff = [&](int f, unsigned (&vo)[4]) {
#pragma unroll
      for (int j = 0; j < 4; ++j) {
        int ix = sidx[j * 8 * F + f];
        ix += (ix >> 31) & V;
        vo[j] = ((unsigned)ix << 8) | ((j & 1) ? sw1 : sw0);
      }
    };
    auto issueA = [&](unsigned (&vo)[4], unsigned sb) {
#pragma unroll
      for (int j = 0; j < 4; ++j) bdma(sb + 16384 + h0 * 128 + lw * 4096 + j * 1024, vo[j], rsT, h0);
#pragma unroll
      for (int j = 0; j < 4; ++j) bdma(sb + 16384 + h1 * 128 + lw * 4096 + j * 1024, vo[j], rsT, h1);
    };
    const int f0 = field_of(0, r, rot), f1 = field_of(1, r, rot), f2 = field_of(2, r, rot);
    issueB(f0, lds0); issueB(f1, lds0 + SLOT); issueB(f2, lds0 + 2 * SLOT);
    waitv<12>();
    unsigned vo[4];
    getoff(f0, vo); issueA(vo, lds0);
    getoff(f1, vo); issueA(vo, lds0 + SLOT);
    getoff(f2, vo); issueA(vo, lds0 + 2 * SLOT);
    int fn = field_of(3, r, rot);
    getoff(fn, vo);
    waitv<16>(); bar();
    waitv<8>();
    unsigned sb = lds0;
    for (int s = 1; s < F - 2; ++s) {
      bar();
      issueB(fn, sb); issueA(vo, sb);
      sb += SLOT; if (sb == lds0 + NSLOT * SLOT) sb = lds0;
      fn = field_of(s + 3 < F ? s + 3 : F - 1, r, rot);
      getoff(fn, vo);
      waitv<12>();
    }
    bar();
    waitv<0>(); bar();
    bar();
    bar();
  } else {
    const int kw = w & 1, mw = w >> 1;
    const int h = lane >> 5, r32 = lane & 31;
    u32x2 pb[8], pw[8];
#pragma unroll
    for (int i = 0; i < 8; ++i) {
      int n = n0 + 8 * i + 4 * h;
      pb[i] = *(const u32x2*)(b1 + n);
      pw[i] = *(const u32x2*)(W2 + n);
    }
    const int G = lane >> 4, q = (lane >> 2) & 3, p = lane & 3;
    unsigned ba[2], aa[4][2];
    {
      int krow = kw * 64 + 8 * (G >> 1) + q;
      int ch = 2 * (G & 1) + (p >> 1);
      ba[0] = (unsigned)(krow * 128 + ((ch + 4 * (q >> 1)) << 4) + 8 * (p & 1));
      ba[1] = (unsigned)(krow * 128 + ((ch + 4 * (1 - (q >> 1))) << 4) + 8 * (p & 1));
      int key = (r32 >> 1) & 7;
#pragma unroll
      for (int j = 0; j < 4; ++j) {
        unsigned a = (unsigned)(16384 + kw * 16384 + (mw * 64 + r32) * 128 + (((2 * j + h) ^ key) << 4));
        aa[j][0] = a + kw * 4096;
        aa[j][1] = a + (1 - kw) * 4096;
      }
    }
    f32x16 acc[2][2] = {};
    bf16x8 wf[2][2], ef[2][2];
    auto rd = [&](lds_char* sp, int j, bf16x8 (&w_)[2], bf16x8 (&e_)[2]) {
#pragma unroll
      for (int t = 0; t < 2; ++t) {
        bf16x4 lo = __builtin_amdgcn_ds_read_tr16_b64_v4i16((lds_bf16x4*)(sp + ba[t] + j * 2048));
        bf16x4 hi = __builtin_amdgcn_ds_read_tr16_b64_v4i16((lds_bf16x4*)(sp + ba[t] + j * 2048 + 512));
        w_[t] = __builtin_shufflevector(lo, hi, 0, 1, 2, 3, 4, 5, 6, 7);
        e_[t] = *(lds_bf16x8*)(sp + aa[j][t]);
      }
    };
    auto mm = [&](bf16x8 (&w_)[2], bf16x8 (&e_)[2]) {
#pragma unroll
      for (int t = 0; t < 2; ++t)
#pragma unroll
        for (int m = 0; m < 2; ++m) acc[t][m] = __builtin_amdgcn_mfma_f32_32x32x16_bf16(w_[t], e_[m], acc[t][m], 0, 0, 0);
    };
#define SB() __builtin_amdgcn_sched_barrier(0)
    unsigned so = 0;
    bar_lgkm();
    rd(lds, 0, wf[0], ef[0]);
    for (int s = 0; s < F; ++s) {
      lds_char* sp = lds + so;
      so += SLOT; if (so == NSLOT * SLOT) so = 0;
      SB(); rd(sp, 1, wf[1], ef[1]); SB(); mm(wf[0], ef[0]);
      SB(); rd(sp, 2, wf[0], ef[0]); SB(); mm(wf[1], ef[1]);
      SB(); rd(sp, 3, wf[1], ef[1]); SB(); mm(wf[0], ef[0]);
      SB(); bar_lgkm();
      if (s < F - 1) rd(lds + so, 0, wf[0], ef[0]);
      SB(); mm(wf[1], ef[1]);
    }
    {
      lds_f32x4* dst = (lds_f32x4*)(lds + w * 8192) + lane;
#pragma unroll
      for (int t = 0; t < 2; ++t)
#pragma unroll
        for (int i = 0; i < 4; ++i) {
          f32x16 a = acc[t][1];
          f32x4 v = {a[4 * i], a[4 * i + 1], a[4 * i + 2], a[4 * i + 3]};
          dst[(t * 4 + i) * 64] = v;
        }
    }
    bar_lgkm();
    float sum = 0.f;
    {
      const lds_f32x4* src = (const lds_f32x4*)(lds + (w ^ 1) * 8192) + lane;
#pragma unroll
      for (int t = 0; t < 2; ++t)
#pragma unroll
        for (int i = 0; i < 4; ++i) {
          f32x16 a = acc[t][0];
          f32x4 o = src[(t * 4 + i) * 64];
          u32x2 bb = pb[t * 4 + i], ww = pw[t * 4 + i];
#pragma unroll
          for (int e = 0; e < 4; ++e) {
            float v = rbf(a[4 * i + e] + o[e]);
            unsigned bw = bb[e >> 1], w2 = ww[e >> 1];
            float bv = __uint_as_float((e & 1) ? (bw & 0xffff0000u) : (bw << 16));
            float wv = __uint_as_float((e & 1) ? (w2 & 0xffff0000u) : (w2 << 16));
            v = rbf(v + bv);
            v = v > 0.f ? v : 0.f;
            sum += v * wv;
          }
        }
    }
    sum += __shfl_xor(sum, 32);
    if (h == 0) part[nt * Bsz + m0 + mw * 64 + kw * 32 + r32] = sum;
  }
}

__global__ __launch_bounds__(256) void k2(const float* __restrict__ part, const unsigned short* __restrict__ xw,
                                          const unsigned short* __restrict__ ww, const unsigned short* __restrict__ bw,
                                          const unsigned short* __restrict__ b2, __hip_bfloat16* __restrict__ out) {
  int b = blockIdx.x * 64 + threadIdx.x;
  float s = 0.f;
#pragma unroll
  for (int j = 0; j < 8; ++j) s += part[j * Bsz + b];
  float deep = rbf(rbf(s) + bf2f(b2[0]));
  float wd = 0.f;
#pragma unroll
  for (int j = 0; j < ND; ++j) wd += bf2f(xw[b * ND + j]) * bf2f(ww[j]);
  float wide = rbf(rbf(wd) + bf2f(bw[0]));
  float z = rbf(wide + deep);
  out[b] = __float2bfloat16(1.f / (1.f + __expf(-z)));
}

extern "C" void kernel_launch(void* const* d_in, const int* in_sizes, int n_in, void* d_out, int out_size, void* d_ws,
                              size_t ws_size, hipStream_t stream) {
  const int* idx = (const int*)d_in[0];
  const unsigned short* table = (const unsigned short*)d_in[1];
  const unsigned short* xw = (const unsigned short*)d_in[2];
  const unsigned short* ww = (const unsigned short*)d_in[3];
  const unsigned short* bw = (const unsigned short*)d_in[4];
  const unsigned short* W1 = (const unsigned short*)d_in[5];
  const unsigned short* b1 = (const unsigned short*)d_in[6];
  const unsigned short* W2 = (const unsigned short*)d_in[7];
  const unsigned short* b2 = (const unsigned short*)d_in[8];
  int V = in_sizes[1] / D;
  float* part = (float*)d_ws;
  hipFuncSetAttribute((const void*)k1, hipFuncAttributeMaxDynamicSharedMemorySize, LDS_BYTES);
  k1<<<256, 512, LDS_BYTES, stream>>>(idx, table, W1, b1, W2, part, V);
  k2<<<Bsz / 64, 64, 0, stream>>>(part, xw, ww, bw, b2, (__hip_bfloat16*)d_out);
}
```

```cpp
#include <hip/hip_runtime.h>
#include <hip/hip_bf16.h>
#include <stdint.h>

typedef __attribute__((ext_vector_type(8))) short bf16x8;
typedef __attribute__((ext_vector_type(4))) short bf16x4;
typedef __attribute__((ext_vector_type(16))) float f32x16;
typedef __attribute__((ext_vector_type(4))) float f32x4;
typedef __attribute__((ext_vector_type(4))) unsigned u32x4;
typedef __attribute__((ext_vector_type(2))) unsigned u32x2;
typedef __attribute__((ext_vector_type(4))) int i32x4;

constexpr int Bsz = 4096, F = 26, D = 128, H = 512, ND = 13;
constexpr int SLOT = 49152, NSLOT = 3, IDXOFF = SLOT * NSLOT, LDS_BYTES = IDXOFF + 13312 + 768;

__device__ __forceinline__ float bf2f(unsigned short x) { return __uint_as_float(((unsigned)x) << 16); }
__device__ __forceinline__ float rbf(float x) { return __bfloat162float(__float2bfloat16(x)); }

__device__ __forceinline__ i32x4 mk_rsrc(const void* p, unsigned bytes) {
  unsigned long long a = (unsigned long long)p;
  i32x4 r;
  r[0] = __builtin_amdgcn_readfirstlane((int)(unsigned)a);
  r[1] = __builtin_amdgcn_readfirstlane((int)((unsigned)(a >> 32) & 0xffff));
  r[2] = __builtin_amdgcn_readfirstlane((int)bytes);
  r[3] = 0x00020000;
  return r;
}
__device__ __forceinline__ void bdma(unsigned ldsbase, unsigned voff, i32x4 rs, unsigned soff) {
  asm volatile("s_mov_b32 m0, %0\n\ts_nop 0\n\tbuffer_load_dwordx4 %1, %2, %3 offen lds" ::"s"(ldsbase), "v"(voff), "s"(rs), "s"(soff) : "memory");
}
template <int N> __device__ __forceinline__ void waitv() { asm volatile("s_waitcnt vmcnt(%0)" ::"i"(N) : "memory"); }
__device__ __forceinline__ void bar() { asm volatile("s_barrier" ::: "memory"); }
__device__ __forceinline__ void bar_lgkm() { asm volatile("s_waitcnt lgkmcnt(0)\n\ts_barrier" ::: "memory"); }

typedef __attribute__((address_space(3))) char lds_char;
typedef __attribute__((address_space(3))) bf16x8 lds_bf16x8;
typedef __attribute__((address_space(3))) bf16x4 lds_bf16x4;
typedef __attribute__((address_space(3))) int lds_int;
typedef __attribute__((address_space(3))) f32x4 lds_f32x4;

#define ST(k)
__device__ __forceinline__ int field_of(int s, int r, int rot) {
  int f;
  if (s == 0) f = r;
  else {
    int tstar = 22 + ((r + 2) & 3);
    int t = s - 1;
    t += (t >= tstar) ? 1 : 0;
    f = ((t & 3) == r) ? t + 4 : t;
  }
  f += rot;
  return f >= F ? f - F : f;
}

__global__ __launch_bounds__(512, 2) void k1(const int* __restrict__ idx, const unsigned short* __restrict__ table,
                                          const unsigned short* __restrict__ W1, const unsigned short* __restrict__ b1,
                                          const unsigned short* __restrict__ W2, float* __restrict__ part, int V) {
  extern __shared__ __attribute__((aligned(16))) char smem[];
  lds_char* lds = (lds_char*)smem;
  const unsigned lds0 = (unsigned)(uintptr_t)lds;
  const int tid = threadIdx.x, lane = tid & 63;
  const int w = __builtin_amdgcn_readfirstlane(tid >> 6);
  const int bid = blockIdx.x;
  const int xcd = bid & 7, slot = bid >> 3;
  const int ml = slot >> 3, nt = slot & 7;
  const int mt = xcd * 4 + ml;
  const int m0 = mt * 128, n0 = nt * 64;

  if (w >= 4) {
    const int lw = w - 4;
    const int g = lane >> 3, c = lane & 7;
    i32x4 rsI = mk_rsrc(idx, Bsz * F * 4);
    i32x4 rsT = mk_rsrc(table, (unsigned)V * D * 2);
    i32x4 rsW = mk_rsrc(W1, F * D * H * 2);
    asm volatile("s_nop 4");
    {
      unsigned vo = (unsigned)(lane * 16);
      unsigned so = (unsigned)((m0 + lw * 32) * F * 4);
#pragma unroll
      for (int j = 0; j < 4; ++j) bdma(lds0 + IDXOFF + lw * 3328 + j * 1024, vo, rsI, so + j * 1024);
    }
    const int r = (ml - nt) & 3, rot = 3 * xcd;
    const unsigned boff = (unsigned)(((lw * 32 + g) * H + n0) * 2 + ((c ^ (((g >> 1) & 1) << 2)) << 4));
    const unsigned sw0 = (unsigned)((c ^ (g >> 1)) << 4), sw1 = (unsigned)((c ^ (4 + (g >> 1))) << 4);
    const unsigned h0 = (unsigned)((nt >> 2) * 128), h1 = 128u - h0;
    auto issueB = [&](int f, unsigned sb) {
#pragma unroll
      for (int j = 0; j < 4; ++j) bdma(sb + lw * 4096 + j * 1024, boff, rsW, (unsigned)((f * 128 + j * 8) * H * 2));
    };
    const lds_int* sidx = (const lds_int*)(lds + IDXOFF) + (lw * 32 + g) * F;
    auto getoff = [&](int f, unsigned (&vo)[4]) {
#pragma unroll
      for (int j = 0; j < 4; ++j) {
        int ix = sidx[j * 8 * F + f];
        ix += (ix >> 31) & V;
        vo[j] = ((unsigned)ix << 8) | ((j & 1) ? sw1 : sw0);
      }
    };
    auto issueA = [&](unsigned (&vo)[4], unsigned sb) {
#pragma unroll
      for (int j = 0; j < 4; ++j) bdma(sb + 16384 + h0 * 128 + lw * 4096 + j * 1024, vo[j], rsT, h0);
#pragma unroll
      for (int j = 0; j < 4; ++j) bdma(sb + 16384 + h1 * 128 + lw * 4096 + j * 1024, vo[j], rsT, h1);
    };
    const int f0 = field_of(0, r, rot), f1 = field_of(1, r, rot), f2 = field_of(2, r, rot);
    issueB(f0, lds0); issueB(f1, lds0 + SLOT); issueB(f2, lds0 + 2 * SLOT);
    waitv<12>();
    unsigned vo[4];
    getoff(f0, vo); issueA(vo, lds0);
    getoff(f1, vo); issueA(vo, lds0 + SLOT);
    getoff(f2, vo); issueA(vo, lds0 + 2 * SLOT);
    int fn = field_of(3, r, rot);
    getoff(fn, vo);
    waitv<16>(); bar();
    if (lw == 0) ST(1);
    waitv<8>();
    unsigned sb = lds0;
    for (int s = 1; s < F - 2; ++s) {
      bar();
      issueB(fn, sb); issueA(vo, sb);
      if (lw == 0) ST(1 + s);
      sb += SLOT; if (sb == lds0 + NSLOT * SLOT) sb = lds0;
      fn = field_of(s + 3 < F ? s + 3 : F - 1, r, rot);
      getoff(fn, vo);
      waitv<12>();
    }
    bar();
    if (lw == 0) ST(25);
    waitv<0>(); bar();
    if (lw == 0) ST(26);
    bar();
    if (lw == 0) ST(27);
    bar();
  } else {
    const int kw = w & 1, mw = w >> 1;
    if (w == 0) ST(0);
    const int h = lane >> 5, r32 = lane & 31;
    u32x2 pb[8], pw[8];
#pragma unroll
    for (int i = 0; i < 8; ++i) {
      int n = n0 + 8 * i + 4 * h;
      pb[i] = *(const u32x2*)(b1 + n);
      pw[i] = *(const u32x2*)(W2 + n);
    }
    const int G = lane >> 4, q = (lane >> 2) & 3, p = lane & 3;
    unsigned ba[2], aa[4][2];
    {
      int krow = kw * 64 + 8 * (G >> 1) + q;
      int ch = 2 * (G & 1) + (p >> 1);
      ba[0] = (unsigned)(krow * 128 + ((ch + 4 * (q >> 1)) << 4) + 8 * (p & 1));
      ba[1] = (unsigned)(krow * 128 + ((ch + 4 * (1 - (q >> 1))) << 4) + 8 * (p & 1));
      int key = (r32 >> 1) & 7;
#pragma unroll
      for (int j = 0; j < 4; ++j) {
        unsigned a = (unsigned)(16384 + kw * 16384 + (mw * 64 + r32) * 128 + (((2 * j + h) ^ key) << 4));
        aa[j][0] = a + kw * 4096;
        aa[j][1] = a + (1 - kw) * 4096;
      }
    }
    f32x16 acc[2][2] = {};
    bf16x8 wf[2][2], ef[2][2];
    auto rd = [&](lds_char* sp, int j, bf16x8 (&w_)[2], bf16x8 (&e_)[2]) {
#pragma unroll
      for (int t = 0; t < 2; ++t) {
        bf16x4 lo = __builtin_amdgcn_ds_read_tr16_b64_v4i16((lds_bf16x4*)(sp + ba[t] + j * 2048));
        bf16x4 hi = __builtin_amdgcn_ds_read_tr16_b64_v4i16((lds_bf16x4*)(sp + ba[t] + j * 2048 + 512));
        w_[t] = __builtin_shufflevector(lo, hi, 0, 1, 2, 3, 4, 5, 6, 7);
        e_[t] = *(lds_bf16x8*)(sp + aa[j][t]);
      }
    };
    auto mm = [&](bf16x8 (&w_)[2], bf16x8 (&e_)[2]) {
#pragma unroll
      for (int t = 0; t < 2; ++t)
#pragma unroll
        for (int m = 0; m < 2; ++m) acc[t][m] = __builtin_amdgcn_mfma_f32_32x32x16_bf16(w_[t], e_[m], acc[t][m], 0, 0, 0);
    };
#define SB() __builtin_amdgcn_sched_barrier(0)
    unsigned so = 0;
    bar_lgkm();
    rd(lds, 0, wf[0], ef[0]);
    for (int s = 0; s < F; ++s) {
      lds_char* sp = lds + so;
      so += SLOT; if (so == NSLOT * SLOT) so = 0;
      SB(); rd(sp, 1, wf[1], ef[1]); SB(); mm(wf[0], ef[0]);
      SB(); rd(sp, 2, wf[0], ef[0]); SB(); mm(wf[1], ef[1]);
      SB(); rd(sp, 3, wf[1], ef[1]); SB(); mm(wf[0], ef[0]);
      SB(); bar_lgkm();
      if (s < F - 1) rd(lds + so, 0, wf[0], ef[0]);
      SB(); mm(wf[1], ef[1]);
    }
    {
      lds_f32x4* dst = (lds_f32x4*)(lds + w * 8192) + lane;
#pragma unroll
      for (int t = 0; t < 2; ++t)
#pragma unroll
        for (int i = 0; i < 4; ++i) {
          f32x16 a = acc[t][1];
          f32x4 v = {a[4 * i], a[4 * i + 1], a[4 * i + 2], a[4 * i + 3]};
          dst[(t * 4 + i) * 64] = v;
        }
    }
    bar_lgkm();
    float sum = 0.f;
    {
      const lds_f32x4* src = (const lds_f32x4*)(lds + (w ^ 1) * 8192) + lane;
#pragma unroll
      for (int t = 0; t < 2; ++t)
#pragma unroll
        for (int i = 0; i < 4; ++i) {
          f32x16 a = acc[t][0];
          f32x4 o = src[(t * 4 + i) * 64];
          u32x2 bb = pb[t * 4 + i], ww = pw[t * 4 + i];
#pragma unroll
          for (int e = 0; e < 4; ++e) {
            float v = rbf(a[4 * i + e] + o[e]);
            unsigned bw = bb[e >> 1], w2 = ww[e >> 1];
            float bv = __uint_as_float((e & 1) ? (bw & 0xffff0000u) : (bw << 16));
            float wv = __uint_as_float((e & 1) ? (w2 & 0xffff0000u) : (w2 << 16));
            v = rbf(v + bv);
            v = v > 0.f ? v : 0.f;
            sum += v * wv;
          }
        }
    }
    sum += __shfl_xor(sum, 32);
    if (h == 0) part[nt * Bsz + m0 + mw * 64 + kw * 32 + r32] = sum;
    if (w == 0) ST(30);
  }
}

__global__ __launch_bounds__(256) void k2(const float* __restrict__ part, const unsigned short* __restrict__ xw,
                                          const unsigned short* __restrict__ ww, const unsigned short* __restrict__ bw,
                                          const unsigned short* __restrict__ b2, __hip_bfloat16* __restrict__ out) {
  int b = blockIdx.x * 64 + threadIdx.x;
  float s = 0.f;
#pragma unroll
  for (int j = 0; j < 8; ++j) s += part[j * Bsz + b];
  float deep = rbf(rbf(s) + bf2f(b2[0]));
  float wd = 0.f;
#pragma unroll
  for (int j = 0; j < ND; ++j) wd += bf2f(xw[b * ND + j]) * bf2f(ww[j]);
  float wide = rbf(rbf(wd) + bf2f(bw[0]));
  float z = rbf(wide + deep);
  out[b] = __float2bfloat16(1.f / (1.f + __expf(-z)));
}

extern "C" void kernel_launch(void* const* d_in, const int* in_sizes, int n_in, void* d_out, int out_size, void* d_ws,
                              size_t ws_size, hipStream_t stream) {
  const int* idx = (const int*)d_in[0];
  const unsigned short* table = (const unsigned short*)d_in[1];
  const unsigned short* xw = (const unsigned short*)d_in[2];
  const unsigned short* ww = (const unsigned short*)d_in[3];
  const unsigned short* bw = (const unsigned short*)d_in[4];
  const unsigned short* W1 = (const unsigned short*)d_in[5];
  const unsigned short* b1 = (const unsigned short*)d_in[6];
  const unsigned short* W2 = (const unsigned short*)d_in[7];
  const unsigned short* b2 = (const unsigned short*)d_in[8];
  int V = in_sizes[1] / D;
  float* part = (float*)d_ws;
  hipFuncSetAttribute((const void*)k1, hipFuncAttributeMaxDynamicSharedMemorySize, LDS_BYTES);
  k1<<<256, 512, LDS_BYTES, stream>>>(idx, table, W1, b1, W2, part, V);
  k2<<<Bsz / 64, 64, 0, stream>>>(part, xw, ww, bw, b2, (__hip_bfloat16*)d_out);
}
```

```cpp
#define STAMP
#include <hip/hip_runtime.h>
#include <hip/hip_bf16.h>
#include <stdint.h>

typedef __attribute__((ext_vector_type(8))) short bf16x8;
typedef __attribute__((ext_vector_type(4))) short bf16x4;
typedef __attribute__((ext_vector_type(2))) short s16x2;
typedef __attribute__((ext_vector_type(2))) __bf16 b16x2;
typedef __attribute__((ext_vector_type(2))) float f32x2;
typedef __attribute__((ext_vector_type(4))) float f32x4;
typedef __attribute__((ext_vector_type(32))) float f32x32;
typedef __attribute__((ext_vector_type(4))) unsigned u32x4;
typedef __attribute__((ext_vector_type(2))) unsigned u32x2;
typedef __attribute__((ext_vector_type(4))) int i32x4;

#define NP1 11
#define SCHED 0
#define DR 2
#define PL 3
#define XROT 3

constexpr int Bsz = 4096, F = 26, D = 128, H = 512, ND = 13;
constexpr int NP2 = F - 2 * NP1, NS = 2 * (NP1 + NP2), NS1 = 2 * NP1;
constexpr int SLOT = 32768, NSLOT = 4, IDXOFF = SLOT * NSLOT, LDS_BYTES = IDXOFF + 13312 + 768;
constexpr unsigned WS_EPOCH = 0, WS_FLAG = 4096, WS_GRAN = 65536, WS_PAY = 2u << 20, WS_END = WS_PAY + 256u * 32768u;
constexpr int SPIN_MAX = 1 << 16;

__device__ __forceinline__ float bf2f(unsigned short x) { return __uint_as_float(((unsigned)x) << 16); }
__device__ __forceinline__ float rbf(float x) { return __bfloat162float(__float2bfloat16(x)); }

__device__ __forceinline__ i32x4 mk_rsrc(const void* p, unsigned bytes) {
  unsigned long long a = (unsigned long long)p;
  i32x4 r;
  r[0] = __builtin_amdgcn_readfirstlane((int)(unsigned)a);
  r[1] = __builtin_amdgcn_readfirstlane((int)((unsigned)(a >> 32) & 0xffff));
  r[2] = __builtin_amdgcn_readfirstlane((int)bytes);
  r[3] = 0x00020000;
  return r;
}
__device__ __forceinline__ void bdma(unsigned ldsbase, unsigned voff, i32x4 rs, unsigned soff) {
  asm volatile("s_mov_b32 m0, %0\n\ts_nop 0\n\tbuffer_load_dwordx4 %1, %2, %3 offen lds" ::"s"(ldsbase), "v"(voff), "s"(rs), "s"(soff) : "memory");
}
template <int N> __device__ __forceinline__ void waitv() { asm volatile("s_waitcnt vmcnt(%0)" ::"i"(N) : "memory"); }
__device__ __forceinline__ void bar() { asm volatile("s_barrier" ::: "memory"); }
__device__ __forceinline__ void bar_lgkm() { asm volatile("s_waitcnt lgkmcnt(0)\n\ts_barrier" ::: "memory"); }

typedef __attribute__((address_space(3))) char lds_char;
typedef __attribute__((address_space(3))) bf16x8 lds_bf16x8;
typedef __attribute__((address_space(3))) bf16x4 lds_bf16x4;
typedef __attribute__((address_space(3))) int lds_int;

#define ST(k) do { unsigned long long _a = __builtin_amdgcn_s_memrealtime(), _b = __builtin_amdgcn_s_memtime(); if (lane == 0) { ((unsigned long long*)(ws + (16 << 20)))[bid * 40 + (k)] = _a; ((unsigned long long*)(ws + (17 << 20)))[bid * 40 + (k)] = _b; } } while (0)

__device__ __forceinline__ int sched(int k, int r) {
  if (k == 0) return r;
  const int tstar = NS - 4 + ((r - NS) & 3);
  int t = k - 1;
  t += (t >= tstar) ? 1 : 0;
  return ((t & 3) == r) ? t + 4 : t;
}

__global__ __launch_bounds__(512, 2) void k1(const int* __restrict__ idx, const unsigned short* __restrict__ table,
                                             const unsigned short* __restrict__ xw, const unsigned short* __restrict__ ww,
                                             const unsigned short* __restrict__ bw, const unsigned short* __restrict__ W1,
                                             const unsigned short* __restrict__ b1, const unsigned short* __restrict__ W2,
                                             const unsigned short* __restrict__ b2, unsigned short* __restrict__ out,
                                             char* ws, int V) {
  extern __shared__ __attribute__((aligned(16))) char smem[];
  lds_char* lds = (lds_char*)smem;
  const unsigned lds0 = (unsigned)(uintptr_t)lds;
  const int tid = threadIdx.x, lane = tid & 63;
  const int w = __builtin_amdgcn_readfirstlane(tid >> 6);
  const int bid = blockIdx.x;
  const int xcd = bid & 7, slot = bid >> 3;
  const int ml = slot >> 3, c8 = slot & 7;
  const int nt = c8 >> 1, p = c8 & 1;
  const int mt = xcd * 4 + ml;
  const int m0 = mt * 128;
  const int nown = nt * 128 + p * 64, noth = nt * 128 + (1 - p) * 64;
  const int r = (ml - nt) & 3;

  if (w >= 4) {
    const int lw = w - 4;
    const int g = lane >> 3, c = lane & 7;
    i32x4 rsI = mk_rsrc(idx, Bsz * F * 4);
    i32x4 rsT = mk_rsrc(table, (unsigned)V * D * 2);
    i32x4 rsW = mk_rsrc(W1, F * D * H * 2);
    asm volatile("s_nop 4");
    {
      unsigned vo = (unsigned)(lane * 16);
      unsigned so = (unsigned)((m0 + lw * 32) * F * 4);
#pragma unroll
      for (int j = 0; j < 4; ++j) bdma(lds0 + IDXOFF + lw * 3328 + j * 1024, vo, rsI, so + j * 1024);
    }
    const int rot = XROT * xcd;
    const unsigned bo0 = (unsigned)((16 * lw + g) * H * 2 + ((c ^ (((g >> 1) & 1) << 1)) << 4));
    const unsigned bo1 = (unsigned)((16 * lw + 8 + g) * H * 2 + ((c ^ ((((g >> 1) & 1) | 2) << 1)) << 4));
    const unsigned sw0 = (unsigned)((c ^ (g >> 1)) << 4), sw1 = (unsigned)((c ^ (4 + (g >> 1))) << 4);
    auto field = [&](int u) {
      int i = u >> 1;
      int f = (i < NP1 ? p * NP1 + i : NP1 + i) + rot;
      f = f >= F ? f - F : f;
      return f;
    };
    auto issueB = [&](int u, int f, unsigned sb) {
      const unsigned so = (unsigned)((f * 128 + (u & 1) * 64) * H * 2);
      bdma(sb + lw * 2048, bo0, rsW, so + nown * 2);
      bdma(sb + lw * 2048 + 1024, bo1, rsW, so + nown * 2);
      if (u < NS1) {
        bdma(sb + 8192 + lw * 2048, bo0, rsW, so + noth * 2);
        bdma(sb + 8192 + lw * 2048 + 1024, bo1, rsW, so + noth * 2);
      }
    };
    const lds_int* sidx = (const lds_int*)(lds + IDXOFF) + (lw * 32 + g) * F;
    auto getoff = [&](int f, unsigned (&vo)[4]) {
#pragma unroll
      for (int j = 0; j < 4; ++j) {
        int ix = sidx[j * 8 * F + f];
        ix += (ix >> 31) & V;
        vo[j] = ((unsigned)ix << 8) | ((j & 1) ? sw1 : sw0);
      }
    };
    auto issueA = [&](int u, unsigned (&vo)[4], unsigned sb) {
      const unsigned so = (unsigned)((u & 1) * 128);
#pragma unroll
      for (int j = 0; j < 4; ++j) bdma(sb + 16384 + lw * 4096 + j * 1024, vo[j], rsT, so);
    };
    const int u0 = sched(0, r), u1 = sched(1, r), u2 = sched(2, r);
    const int f0 = field(u0), f1 = field(u1), f2 = field(u2);
    issueB(u0, f0, lds0); issueB(u1, f1, lds0 + SLOT); issueB(u2, f2, lds0 + 2 * SLOT);
    waitv<12>();
    unsigned vo[4], v1[4], v2[4];
    getoff(f0, vo); getoff(f1, v1); getoff(f2, v2);
    issueA(u0, vo, lds0); issueA(u1, v1, lds0 + SLOT); issueA(u2, v2, lds0 + 2 * SLOT);
    int un = sched(3, r), fn = field(un);
    getoff(fn, vo);
    waitv<8>();
    unsigned sb = lds0 + 3 * SLOT;
    for (int k = 0; k < NS - 3; ++k) {
      bar();
      issueB(un, fn, sb); issueA(un, vo, sb);
      sb += SLOT; if (sb == lds0 + NSLOT * SLOT) sb = lds0;
      un = sched(k + 4 < NS ? k + 4 : NS - 1, r); fn = field(un);
      getoff(fn, vo);
      if (k == 0) waitv<10>(); else waitv<12>();
    }
    bar();
    waitv<6>(); bar();
    waitv<0>(); bar();
  } else {
    const int nq = w & 1, mw = w >> 1;
    const int G = lane >> 4, q = (lane >> 2) & 3, pp = lane & 3, c16 = lane & 15;
    if (w == 0) ST(0);
    const int gid = mt * 8 + c8;
    __amdgpu_buffer_rsrc_t rsS = __builtin_amdgcn_make_buffer_rsrc((void*)ws, 0, (int)WS_END, 0x00020000);
    u32x2 ep = __builtin_bit_cast(u32x2, __builtin_amdgcn_raw_buffer_load_b64(rsS, 0, (int)(WS_EPOCH + mt * 128), 16));
    u32x2 pb[2], pw[2];
#pragma unroll
    for (int b = 0; b < 2; ++b) {
      int n = nown + 32 * nq + 16 * b + 4 * G;
      pb[b] = *(const u32x2*)(b1 + n);
      pw[b] = *(const u32x2*)(W2 + n);
    }
    float wide;
    {
      const int smp = m0 + 16 * c8 + c16;
      float wd = 0.f;
#pragma unroll
      for (int j = 0; j < ND; ++j) wd += bf2f(xw[smp * ND + j]) * bf2f(ww[j]);
      wide = rbf(rbf(wd) + bf2f(bw[0]));
    }
    float b2v = bf2f(b2[0]);
    asm volatile("" : "+v"(wide), "+v"(b2v));
    const unsigned e0 = __builtin_amdgcn_readfirstlane(ep[0]) + 1u, e1 = __builtin_amdgcn_readfirstlane(ep[1]);
    const unsigned tag0 = e0 * 0x9E3779B1u + 0x7F4A7C15u, tag1 = (e0 ^ 0x5bd1e995u) * 0x85EBCA6Bu + e1;
    int e = 0;
    unsigned mask = 0;
    for (int k = 0; k < NS; ++k) { const bool t = sched(k, r) < NS1; e = t ? k : e; mask |= t ? (1u << k) : 0u; }

    unsigned wa[2], ea[2];
    {
      const int key2 = (q >> 1) | ((G & 1) << 1);
#pragma unroll
      for (int b = 0; b < 2; ++b) wa[b] = lds0 + (unsigned)((8 * G + q) * 128 + (((2 * (2 * nq + b) + (pp >> 1)) ^ (key2 << 1)) << 4) + 8 * (pp & 1));
#pragma unroll
      for (int g = 0; g < 2; ++g) ea[g] = lds0 + (unsigned)(16384 + (64 * mw + c16) * 128 + (((4 * g + G) ^ (c16 >> 1)) << 4));
    }
    f32x32 own, oth;
#pragma unroll
    for (int b = 0; b < 2; ++b) {
      const unsigned p0 = pb[b][0], p1 = pb[b][1];
#pragma unroll
      for (int m = 0; m < 4; ++m) {
        own[(b * 4 + m) * 4 + 0] = __uint_as_float(p0 << 16);
        own[(b * 4 + m) * 4 + 1] = __uint_as_float(p0 & 0xffff0000u);
        own[(b * 4 + m) * 4 + 2] = __uint_as_float(p1 << 16);
        own[(b * 4 + m) * 4 + 3] = __uint_as_float(p1 & 0xffff0000u);
      }
    }
    {
      const i32x4 srd = mk_rsrc(ws, WS_END);
      const unsigned pos = WS_PAY + (unsigned)gid * 32768u + (unsigned)w * 8192u;
      const unsigned pop = WS_PAY + (unsigned)(gid ^ 1) * 32768u + (unsigned)w * 8192u;
      const unsigned mfl = WS_FLAG + (unsigned)gid * 128u, pfl = WS_FLAG + (unsigned)(gid ^ 1) * 128u;
      const int kd = e + DR, kdw = (w == 0) ? kd : -1, kp = NS - PL;
      unsigned sk, sso, st0, st1, scnt;
      unsigned long long q0, q1, q2, q3, q4;
#define STAMPASM(i) "s_memrealtime %[q" #i "]\n\ts_waitcnt lgkmcnt(0)\n\t"
      asm volatile(
      "s_nop 4\n\t"
      "v_mov_b32 v96, 0\n\t"
      "v_mov_b32 v97, 0\n\t"
      "v_mov_b32 v98, 0\n\t"
      "v_mov_b32 v99, 0\n\t"
      "v_mov_b32 v100, 0\n\t"
      "v_mov_b32 v101, 0\n\t"
      "v_mov_b32 v102, 0\n\t"
      "v_mov_b32 v103, 0\n\t"
      "v_mov_b32 v104, 0\n\t"
      "v_mov_b32 v105, 0\n\t"
      "v_mov_b32 v106, 0\n\t"
      "v_mov_b32 v107, 0\n\t"
      "v_mov_b32 v108, 0\n\t"
      "v_mov_b32 v109, 0\n\t"
      "v_mov_b32 v110, 0\n\t"
      "v_mov_b32 v111, 0\n\t"
      "v_mov_b32 v112, 0\n\t"
      "v_mov_b32 v113, 0\n\t"
      "v_mov_b32 v114, 0\n\t"
      "v_mov_b32 v115, 0\n\t"
      "v_mov_b32 v116, 0\n\t"
      "v_mov_b32 v117, 0\n\t"
      "v_mov_b32 v118, 0\n\t"
      "v_mov_b32 v119, 0\n\t"
      "v_mov_b32 v120, 0\n\t"
      "v_mov_b32 v121, 0\n\t"
      "v_mov_b32 v122, 0\n\t"
      "v_mov_b32 v123, 0\n\t"
      "v_mov_b32 v124, 0\n\t"
      "v_mov_b32 v125, 0\n\t"
      "v_mov_b32 v126, 0\n\t"
      "v_mov_b32 v127, 0\n\t"
      "v_mov_b32 v192, %[wa0]\n\t"
      "v_mov_b32 v193, %[wa1]\n\t"
      "v_mov_b32 v194, %[ea0]\n\t"
      "v_mov_b32 v195, %[ea1]\n\t"
      "v_mov_b32 v198, %[tagA]\n\t"
      "v_mov_b32 v199, %[tagB]\n\t"
      "s_mov_b32 %[k], 0\n\t"
      "s_mov_b32 %[so], 0\n\t"
      "s_waitcnt vmcnt(0) lgkmcnt(0)\n\t"
      "s_barrier\n\t"
      STAMPASM(0)
      "ds_read_b64_tr_b16 v[128:129], v192 offset:0\n\t"
      "ds_read_b64_tr_b16 v[130:131], v192 offset:512\n\t"
      "ds_read_b128 v[144:147], v194 offset:0\n\t"
      "ds_read_b128 v[148:151], v194 offset:2048\n\t"
      "ds_read_b128 v[152:155], v194 offset:4096\n\t"
      "ds_read_b128 v[156:159], v194 offset:6144\n\t"
      "ds_read_b64_tr_b16 v[132:133], v193 offset:0\n\t"
      "ds_read_b64_tr_b16 v[134:135], v193 offset:512\n\t"
      "ds_read_b64_tr_b16 v[136:137], v192 offset:8192\n\t"
      "ds_read_b64_tr_b16 v[138:139], v192 offset:8704\n\t"
      "ds_read_b64_tr_b16 v[140:141], v193 offset:8192\n\t"
      "ds_read_b64_tr_b16 v[142:143], v193 offset:8704\n\t"
      "L1_%=:\n\t"
      "s_waitcnt lgkmcnt(0)\n\t"
      "v_mfma_f32_16x16x32_bf16 v[64:67], v[128:131], v[144:147], v[64:67]\n\t"
      "ds_read_b64_tr_b16 v[160:161], v192 offset:4096\n\t"
      "ds_read_b64_tr_b16 v[162:163], v192 offset:4608\n\t"
      "v_mfma_f32_16x16x32_bf16 v[68:71], v[128:131], v[148:151], v[68:71]\n\t"
      "ds_read_b128 v[176:179], v195 offset:0\n\t"
      "v_mfma_f32_16x16x32_bf16 v[72:75], v[128:131], v[152:155], v[72:75]\n\t"
      "ds_read_b128 v[180:183], v195 offset:2048\n\t"
      "v_mfma_f32_16x16x32_bf16 v[76:79], v[128:131], v[156:159], v[76:79]\n\t"
      "ds_read_b128 v[184:187], v195 offset:4096\n\t"
      "v_mfma_f32_16x16x32_bf16 v[80:83], v[132:135], v[144:147], v[80:83]\n\t"
      "ds_read_b128 v[188:191], v195 offset:6144\n\t"
      "v_mfma_f32_16x16x32_bf16 v[84:87], v[132:135], v[148:151], v[84:87]\n\t"
      "ds_read_b64_tr_b16 v[164:165], v193 offset:4096\n\t"
      "ds_read_b64_tr_b16 v[166:167], v193 offset:4608\n\t"
      "v_mfma_f32_16x16x32_bf16 v[88:91], v[132:135], v[152:155], v[88:91]\n\t"
      "ds_read_b64_tr_b16 v[168:169], v192 offset:12288\n\t"
      "ds_read_b64_tr_b16 v[170:171], v192 offset:12800\n\t"
      "v_mfma_f32_16x16x32_bf16 v[92:95], v[132:135], v[156:159], v[92:95]\n\t"
      "ds_read_b64_tr_b16 v[172:173], v193 offset:12288\n\t"
      "ds_read_b64_tr_b16 v[174:175], v193 offset:12800\n\t"
      "v_mfma_f32_16x16x32_bf16 v[96:99], v[136:139], v[144:147], v[96:99]\n\t"
      "s_add_u32 %[so], %[so], 0x8000\n\t"
      "s_and_b32 %[so], %[so], 0x1ffff\n\t"
      "v_mfma_f32_16x16x32_bf16 v[100:103], v[136:139], v[148:151], v[100:103]\n\t"
      "v_add_u32 v192, %[so], %[wa0]\n\t"
      "v_add_u32 v193, %[so], %[wa1]\n\t"
      "v_mfma_f32_16x16x32_bf16 v[104:107], v[136:139], v[152:155], v[104:107]\n\t"
      "v_add_u32 v194, %[so], %[ea0]\n\t"
      "v_add_u32 v195, %[so], %[ea1]\n\t"
      "v_mfma_f32_16x16x32_bf16 v[108:111], v[136:139], v[156:159], v[108:111]\n\t"
      "v_mfma_f32_16x16x32_bf16 v[112:115], v[140:143], v[144:147], v[112:115]\n\t"
      "v_mfma_f32_16x16x32_bf16 v[116:119], v[140:143], v[148:151], v[116:119]\n\t"
      "v_mfma_f32_16x16x32_bf16 v[120:123], v[140:143], v[152:155], v[120:123]\n\t"
      "v_mfma_f32_16x16x32_bf16 v[124:127], v[140:143], v[156:159], v[124:127]\n\t"
      "s_waitcnt lgkmcnt(0)\n\t"
      "s_barrier\n\t"
      "v_mfma_f32_16x16x32_bf16 v[64:67], v[160:163], v[176:179], v[64:67]\n\t"
      "ds_read_b64_tr_b16 v[128:129], v192 offset:0\n\t"
      "ds_read_b64_tr_b16 v[130:131], v192 offset:512\n\t"
      "v_mfma_f32_16x16x32_bf16 v[68:71], v[160:163], v[180:183], v[68:71]\n\t"
      "ds_read_b128 v[144:147], v194 offset:0\n\t"
      "v_mfma_f32_16x16x32_bf16 v[72:75], v[160:163], v[184:187], v[72:75]\n\t"
      "ds_read_b128 v[148:151], v194 offset:2048\n\t"
      "v_mfma_f32_16x16x32_bf16 v[76:79], v[160:163], v[188:191], v[76:79]\n\t"
      "ds_read_b128 v[152:155], v194 offset:4096\n\t"
      "v_mfma_f32_16x16x32_bf16 v[80:83], v[164:167], v[176:179], v[80:83]\n\t"
      "ds_read_b128 v[156:159], v194 offset:6144\n\t"
      "v_mfma_f32_16x16x32_bf16 v[84:87], v[164:167], v[180:183], v[84:87]\n\t"
      "ds_read_b64_tr_b16 v[132:133], v193 offset:0\n\t"
      "ds_read_b64_tr_b16 v[134:135], v193 offset:512\n\t"
      "v_mfma_f32_16x16x32_bf16 v[88:91], v[164:167], v[184:187], v[88:91]\n\t"
      "ds_read_b64_tr_b16 v[136:137], v192 offset:8192\n\t"
      "ds_read_b64_tr_b16 v[138:139], v192 offset:8704\n\t"
      "v_mfma_f32_16x16x32_bf16 v[92:95], v[164:167], v[188:191], v[92:95]\n\t"
      "ds_read_b64_tr_b16 v[140:141], v193 offset:8192\n\t"
      "ds_read_b64_tr_b16 v[142:143], v193 offset:8704\n\t"
      "v_mfma_f32_16x16x32_bf16 v[96:99], v[168:171], v[176:179], v[96:99]\n\t"
      "s_add_u32 %[k], %[k], 1\n\t"
      "v_mfma_f32_16x16x32_bf16 v[100:103], v[168:171], v[180:183], v[100:103]\n\t"
      "s_bitcmp1_b32 %[mask], %[k]\n\t"
      "v_mfma_f32_16x16x32_bf16 v[104:107], v[168:171], v[184:187], v[104:107]\n\t"
      "v_mfma_f32_16x16x32_bf16 v[108:111], v[168:171], v[188:191], v[108:111]\n\t"
      "v_mfma_f32_16x16x32_bf16 v[112:115], v[172:175], v[176:179], v[112:115]\n\t"
      "v_mfma_f32_16x16x32_bf16 v[116:119], v[172:175], v[180:183], v[116:119]\n\t"
      "v_mfma_f32_16x16x32_bf16 v[120:123], v[172:175], v[184:187], v[120:123]\n\t"
      "v_mfma_f32_16x16x32_bf16 v[124:127], v[172:175], v[188:191], v[124:127]\n\t"
      "s_cbranch_scc1 L1_%=\n\t"
      "L2_%=:\n\t"
      "s_cmp_gt_u32 %[k], %[e]\n\t"
      "s_cbranch_scc1 L3_%=\n\t"
      "s_waitcnt lgkmcnt(0)\n\t"
      "v_mfma_f32_16x16x32_bf16 v[64:67], v[128:131], v[144:147], v[64:67]\n\t"
      "ds_read_b64_tr_b16 v[160:161], v192 offset:4096\n\t"
      "ds_read_b64_tr_b16 v[162:163], v192 offset:4608\n\t"
      "v_mfma_f32_16x16x32_bf16 v[68:71], v[128:131], v[148:151], v[68:71]\n\t"
      "ds_read_b128 v[176:179], v195 offset:0\n\t"
      "v_mfma_f32_16x16x32_bf16 v[72:75], v[128:131], v[152:155], v[72:75]\n\t"
      "ds_read_b128 v[180:183], v195 offset:2048\n\t"
      "v_mfma_f32_16x16x32_bf16 v[76:79], v[128:131], v[156:159], v[76:79]\n\t"
      "ds_read_b128 v[184:187], v195 offset:4096\n\t"
      "v_mfma_f32_16x16x32_bf16 v[80:83], v[132:135], v[144:147], v[80:83]\n\t"
      "ds_read_b128 v[188:191], v195 offset:6144\n\t"
      "v_mfma_f32_16x16x32_bf16 v[84:87], v[132:135], v[148:151], v[84:87]\n\t"
      "ds_read_b64_tr_b16 v[164:165], v193 offset:4096\n\t"
      "ds_read_b64_tr_b16 v[166:167], v193 offset:4608\n\t"
      "v_mfma_f32_16x16x32_bf16 v[88:91], v[132:135], v[152:155], v[88:91]\n\t"
      "s_add_u32 %[so], %[so], 0x8000\n\t"
      "s_and_b32 %[so], %[so], 0x1ffff\n\t"
      "v_add_u32 v192, %[so], %[wa0]\n\t"
      "v_add_u32 v193, %[so], %[wa1]\n\t"
      "v_mfma_f32_16x16x32_bf16 v[92:95], v[132:135], v[156:159], v[92:95]\n\t"
      "v_add_u32 v194, %[so], %[ea0]\n\t"
      "v_add_u32 v195, %[so], %[ea1]\n\t"
      "s_waitcnt lgkmcnt(0)\n\t"
      "s_barrier\n\t"
      "v_mfma_f32_16x16x32_bf16 v[64:67], v[160:163], v[176:179], v[64:67]\n\t"
      "ds_read_b64_tr_b16 v[128:129], v192 offset:0\n\t"
      "ds_read_b64_tr_b16 v[130:131], v192 offset:512\n\t"
      "v_mfma_f32_16x16x32_bf16 v[68:71], v[160:163], v[180:183], v[68:71]\n\t"
      "ds_read_b128 v[144:147], v194 offset:0\n\t"
      "v_mfma_f32_16x16x32_bf16 v[72:75], v[160:163], v[184:187], v[72:75]\n\t"
      "ds_read_b128 v[148:151], v194 offset:2048\n\t"
      "v_mfma_f32_16x16x32_bf16 v[76:79], v[160:163], v[188:191], v[76:79]\n\t"
      "ds_read_b128 v[152:155], v194 offset:4096\n\t"
      "v_mfma_f32_16x16x32_bf16 v[80:83], v[164:167], v[176:179], v[80:83]\n\t"
      "ds_read_b128 v[156:159], v194 offset:6144\n\t"
      "v_mfma_f32_16x16x32_bf16 v[84:87], v[164:167], v[180:183], v[84:87]\n\t"
      "ds_read_b64_tr_b16 v[132:133], v193 offset:0\n\t"
      "ds_read_b64_tr_b16 v[134:135], v193 offset:512\n\t"
      "v_mfma_f32_16x16x32_bf16 v[88:91], v[164:167], v[184:187], v[88:91]\n\t"
      "ds_read_b64_tr_b16 v[136:137], v192 offset:8192\n\t"
      "ds_read_b64_tr_b16 v[138:139], v192 offset:8704\n\t"
      "v_mfma_f32_16x16x32_bf16 v[92:95], v[164:167], v[188:191], v[92:95]\n\t"
      "ds_read_b64_tr_b16 v[140:141], v193 offset:8192\n\t"
      "ds_read_b64_tr_b16 v[142:143], v193 offset:8704\n\t"
      "s_add_u32 %[k], %[k], 1\n\t"
      "s_bitcmp1_b32 %[mask], %[k]\n\t"
      "s_cbranch_scc1 L1_%=\n\t"
      "s_branch L2_%=\n\t"
      "L3_%=:\n\t"
      STAMPASM(1)
      "s_add_u32 %[t0], %[pos], 0x1000\n\t"
      "s_nop 7\n\t"
      "buffer_store_dwordx4 v[96:99], %[l16], %[srd], %[pos] offen offset:0 sc1\n\t"
      "buffer_store_dwordx4 v[100:103], %[l16], %[srd], %[pos] offen offset:1024 sc1\n\t"
      "buffer_store_dwordx4 v[104:107], %[l16], %[srd], %[pos] offen offset:2048 sc1\n\t"
      "buffer_store_dwordx4 v[108:111], %[l16], %[srd], %[pos] offen offset:3072 sc1\n\t"
      "buffer_store_dwordx4 v[112:115], %[l16], %[srd], %[t0] offen offset:0 sc1\n\t"
      "buffer_store_dwordx4 v[116:119], %[l16], %[srd], %[t0] offen offset:1024 sc1\n\t"
      "buffer_store_dwordx4 v[120:123], %[l16], %[srd], %[t0] offen offset:2048 sc1\n\t"
      "buffer_store_dwordx4 v[124:127], %[l16], %[srd], %[t0] offen offset:3072 sc1\n\t"
      "L4_%=:\n\t"
      "s_cmp_lg_u32 %[k], %[kp]\n\t"
      "s_cbranch_scc1 L6_%=\n\t"
      STAMPASM(2)
      "s_mov_b32 %[cnt], 0x10000\n\t"
      "L5_%=:\n\t"
      "buffer_load_dwordx2 v[196:197], off, %[srd], %[pfl] sc1\n\t"
      "s_sub_u32 %[cnt], %[cnt], 1\n\t"
      "s_cbranch_scc1 L7_%=\n\t"
      "s_waitcnt vmcnt(0)\n\t"
      "v_readfirstlane_b32 %[t0], v196\n\t"
      "v_readfirstlane_b32 %[t1], v197\n\t"
      "s_nop 1\n\t"
      "s_xor_b32 %[t0], %[t0], %[tagA]\n\t"
      "s_xor_b32 %[t1], %[t1], %[tagB]\n\t"
      "s_or_b32 %[t0], %[t0], %[t1]\n\t"
      "s_cbranch_scc1 L5_%=\n\t"
      "L7_%=:\n\t"
      STAMPASM(3)
      "s_add_u32 %[t0], %[pop], 0x1000\n\t"
      "buffer_load_dwordx4 v[96:99], %[l16], %[srd], %[pop] offen offset:0 sc1\n\t"
      "buffer_load_dwordx4 v[100:103], %[l16], %[srd], %[pop] offen offset:1024 sc1\n\t"
      "buffer_load_dwordx4 v[104:107], %[l16], %[srd], %[pop] offen offset:2048 sc1\n\t"
      "buffer_load_dwordx4 v[108:111], %[l16], %[srd], %[pop] offen offset:3072 sc1\n\t"
      "buffer_load_dwordx4 v[112:115], %[l16], %[srd], %[t0] offen offset:0 sc1\n\t"
      "buffer_load_dwordx4 v[116:119], %[l16], %[srd], %[t0] offen offset:1024 sc1\n\t"
      "buffer_load_dwordx4 v[120:123], %[l16], %[srd], %[t0] offen offset:2048 sc1\n\t"
      "buffer_load_dwordx4 v[124:127], %[l16], %[srd], %[t0] offen offset:3072 sc1\n\t"
      "L6_%=:\n\t"
      "s_waitcnt lgkmcnt(5)\n\t"
      "v_mfma_f32_16x16x32_bf16 v[64:67], v[128:131], v[144:147], v[64:67]\n\t"
      "ds_read_b64_tr_b16 v[160:161], v192 offset:4096\n\t"
      "ds_read_b64_tr_b16 v[162:163], v192 offset:4608\n\t"
      "s_waitcnt lgkmcnt(6)\n\t"
      "v_mfma_f32_16x16x32_bf16 v[68:71], v[128:131], v[148:151], v[68:71]\n\t"
      "ds_read_b128 v[176:179], v195 offset:0\n\t"
      "s_waitcnt lgkmcnt(6)\n\t"
      "v_mfma_f32_16x16x32_bf16 v[72:75], v[128:131], v[152:155], v[72:75]\n\t"
      "ds_read_b128 v[180:183], v195 offset:2048\n\t"
      "s_waitcnt lgkmcnt(6)\n\t"
      "v_mfma_f32_16x16x32_bf16 v[76:79], v[128:131], v[156:159], v[76:79]\n\t"
      "ds_read_b128 v[184:187], v195 offset:4096\n\t"
      "s_waitcnt lgkmcnt(5)\n\t"
      "v_mfma_f32_16x16x32_bf16 v[80:83], v[132:135], v[144:147], v[80:83]\n\t"
      "ds_read_b128 v[188:191], v195 offset:6144\n\t"
      "v_mfma_f32_16x16x32_bf16 v[84:87], v[132:135], v[148:151], v[84:87]\n\t"
      "ds_read_b64_tr_b16 v[164:165], v193 offset:4096\n\t"
      "ds_read_b64_tr_b16 v[166:167], v193 offset:4608\n\t"
      "v_mfma_f32_16x16x32_bf16 v[88:91], v[132:135], v[152:155], v[88:91]\n\t"
      "s_add_u32 %[so], %[so], 0x8000\n\t"
      "s_and_b32 %[so], %[so], 0x1ffff\n\t"
      "v_add_u32 v192, %[so], %[wa0]\n\t"
      "v_add_u32 v193, %[so], %[wa1]\n\t"
      "v_mfma_f32_16x16x32_bf16 v[92:95], v[132:135], v[156:159], v[92:95]\n\t"
      "v_add_u32 v194, %[so], %[ea0]\n\t"
      "v_add_u32 v195, %[so], %[ea1]\n\t"
      "s_cmp_eq_u32 %[k], %[nsm1]\n\t"
      "s_cbranch_scc1 L9_%=\n\t"
      "s_cmp_lg_u32 %[k], %[kd]\n\t"
      "s_cbranch_scc1 L8_%=\n\t"
      "s_waitcnt vmcnt(0)\n\t"
      "L8_%=:\n\t"
      "s_waitcnt lgkmcnt(0)\n\t"
      "s_barrier\n\t"
      "s_cmp_lg_u32 %[k], %[kdw]\n\t"
      "s_cbranch_scc1 L10_%=\n\t"
      "s_mov_b64 exec, 1\n\t"
      "buffer_store_dwordx2 v[198:199], off, %[srd], %[mfl] sc1\n\t"
      "s_mov_b64 exec, -1\n\t"
      "L10_%=:\n\t"
      "v_mfma_f32_16x16x32_bf16 v[64:67], v[160:163], v[176:179], v[64:67]\n\t"
      "ds_read_b64_tr_b16 v[128:129], v192 offset:0\n\t"
      "ds_read_b64_tr_b16 v[130:131], v192 offset:512\n\t"
      "v_mfma_f32_16x16x32_bf16 v[68:71], v[160:163], v[180:183], v[68:71]\n\t"
      "ds_read_b128 v[144:147], v194 offset:0\n\t"
      "v_mfma_f32_16x16x32_bf16 v[72:75], v[160:163], v[184:187], v[72:75]\n\t"
      "ds_read_b128 v[148:151], v194 offset:2048\n\t"
      "v_mfma_f32_16x16x32_bf16 v[76:79], v[160:163], v[188:191], v[76:79]\n\t"
      "ds_read_b128 v[152:155], v194 offset:4096\n\t"
      "v_mfma_f32_16x16x32_bf16 v[80:83], v[164:167], v[176:179], v[80:83]\n\t"
      "ds_read_b128 v[156:159], v194 offset:6144\n\t"
      "v_mfma_f32_16x16x32_bf16 v[84:87], v[164:167], v[180:183], v[84:87]\n\t"
      "ds_read_b64_tr_b16 v[132:133], v193 offset:0\n\t"
      "ds_read_b64_tr_b16 v[134:135], v193 offset:512\n\t"
      "v_mfma_f32_16x16x32_bf16 v[88:91], v[164:167], v[184:187], v[88:91]\n\t"
      "s_add_u32 %[k], %[k], 1\n\t"
      "v_mfma_f32_16x16x32_bf16 v[92:95], v[164:167], v[188:191], v[92:95]\n\t"
      "s_branch L4_%=\n\t"
      "L9_%=:\n\t"
      "s_waitcnt lgkmcnt(5)\n\t"
      "v_mfma_f32_16x16x32_bf16 v[64:67], v[160:163], v[176:179], v[64:67]\n\t"
      "s_waitcnt lgkmcnt(4)\n\t"
      "v_mfma_f32_16x16x32_bf16 v[68:71], v[160:163], v[180:183], v[68:71]\n\t"
      "s_waitcnt lgkmcnt(3)\n\t"
      "v_mfma_f32_16x16x32_bf16 v[72:75], v[160:163], v[184:187], v[72:75]\n\t"
      "s_waitcnt lgkmcnt(2)\n\t"
      "v_mfma_f32_16x16x32_bf16 v[76:79], v[160:163], v[188:191], v[76:79]\n\t"
      "s_waitcnt lgkmcnt(0)\n\t"
      "v_mfma_f32_16x16x32_bf16 v[80:83], v[164:167], v[176:179], v[80:83]\n\t"
      "v_mfma_f32_16x16x32_bf16 v[84:87], v[164:167], v[180:183], v[84:87]\n\t"
      "v_mfma_f32_16x16x32_bf16 v[88:91], v[164:167], v[184:187], v[88:91]\n\t"
      "v_mfma_f32_16x16x32_bf16 v[92:95], v[164:167], v[188:191], v[92:95]\n\t"
      STAMPASM(4)
      "s_waitcnt vmcnt(0)\n\t"
      "s_nop 7\n\t"
      : "+{v[64:95]}"(own), "=&{v[96:127]}"(oth), [k] "=&s"(sk), [so] "=&s"(sso), [t0] "=&s"(st0), [t1] "=&s"(st1), [cnt] "=&s"(scnt)
        , [q0] "=&s"(q0), [q1] "=&s"(q1), [q2] "=&s"(q2), [q3] "=&s"(q3), [q4] "=&s"(q4)
      : [wa0] "v"(wa[0]), [wa1] "v"(wa[1]), [ea0] "v"(ea[0]), [ea1] "v"(ea[1]), [l16] "v"(lane * 16), [srd] "s"(srd), [mask] "s"(mask), [e] "s"(e),
        [kd] "s"(kd), [kdw] "s"(kdw), [kp] "s"(kp), [pos] "s"(pos), [pop] "s"(pop), [mfl] "s"(mfl), [pfl] "s"(pfl), [tagA] "s"(tag1), [tagB] "s"(tag0),
        [nsm1] "i"(NS - 1)
      : "memory", "scc", "v128", "v129", "v130", "v131", "v132", "v133", "v134", "v135", "v136", "v137", "v138", "v139", "v140", "v141", "v142", "v143", "v144", "v145", "v146", "v147", "v148", "v149", "v150", "v151", "v152", "v153", "v154", "v155", "v156", "v157", "v158", "v159", "v160", "v161", "v162", "v163", "v164", "v165", "v166", "v167", "v168", "v169", "v170", "v171", "v172", "v173", "v174", "v175", "v176", "v177", "v178", "v179", "v180", "v181", "v182", "v183", "v184", "v185", "v186", "v187", "v188", "v189", "v190", "v191", "v192", "v193", "v194", "v195", "v196", "v197", "v198", "v199");
      if (w == 0 && lane == 0) {
        unsigned long long* sp = (unsigned long long*)(ws + (16 << 20)) + bid * 40;
        sp[1] = q0; sp[2] = q1; sp[3] = q2; sp[4] = q3; sp[5] = q4;
      }
    }
    if (w == 0) ST(6);
    float s[4];
#pragma unroll
    for (int m = 0; m < 4; ++m) {
      float a = 0.f;
#pragma unroll
      for (int b = 0; b < 2; ++b) {
        f32x4 v;
#pragma unroll
        for (int i = 0; i < 4; ++i) v[i] = own[(b * 4 + m) * 4 + i] + oth[(b * 4 + m) * 4 + i];
        const unsigned w0 = pw[b][0], w1 = pw[b][1];
        const s16x2 z = {0, 0};
        s16x2 h0 = __builtin_elementwise_max(__builtin_bit_cast(s16x2, __builtin_convertvector((f32x2){v[0], v[1]}, b16x2)), z);
        s16x2 h1 = __builtin_elementwise_max(__builtin_bit_cast(s16x2, __builtin_convertvector((f32x2){v[2], v[3]}, b16x2)), z);
        a = __builtin_amdgcn_fdot2_f32_bf16(__builtin_bit_cast(b16x2, h0), __builtin_bit_cast(b16x2, w0), a, false);
        a = __builtin_amdgcn_fdot2_f32_bf16(__builtin_bit_cast(b16x2, h1), __builtin_bit_cast(b16x2, w1), a, false);
      }
      s[m] = a;
    }
    float tot;
    {
      auto x = __builtin_amdgcn_permlane32_swap(__float_as_uint(s[0]), __float_as_uint(s[2]), false, false);
      auto y = __builtin_amdgcn_permlane32_swap(__float_as_uint(s[1]), __float_as_uint(s[3]), false, false);
      float t0 = __uint_as_float(x[0]) + __uint_as_float(x[1]);
      float t1 = __uint_as_float(y[0]) + __uint_as_float(y[1]);
      auto z = __builtin_amdgcn_permlane16_swap(__float_as_uint(t0), __float_as_uint(t1), false, false);
      tot = __uint_as_float(z[0]) + __uint_as_float(z[1]);
    }
    {
      u32x4 gv = {__float_as_uint(tot), tag0, __float_as_uint(tot), tag1};
      const unsigned go = (unsigned)(G * 4096 + (c8 * 2 + nq) * 256 + c16 * 16);
      __builtin_amdgcn_raw_buffer_store_b128(gv, rsS, (int)go, (int)(WS_GRAN + (unsigned)(mt * 8 + 4 * mw) * 4096u), 16);
    }
    if (w == 0) {
      ST(7);
      float acc = 0.f;
      for (int spin = 0; spin < SPIN_MAX; ++spin) {
        bool ok = true;
        acc = 0.f;
        asm volatile("" ::: "memory");
#pragma unroll
        for (int i = 0; i < 4; ++i) {
          u32x4 gv = __builtin_amdgcn_raw_buffer_load_b128(rsS, lane * 16 + i * 1024, (int)(WS_GRAN + (unsigned)gid * 4096u), 16);
          ok = ok && gv[1] == tag0 && gv[3] == tag1 && gv[0] == gv[2];
          acc += __uint_as_float(gv[0]);
        }
        if (__all(ok)) break;
      }
      auto x = __builtin_amdgcn_permlane32_swap(__float_as_uint(acc), __float_as_uint(acc), false, false);
      acc = __uint_as_float(x[0]) + __uint_as_float(x[1]);
      auto y = __builtin_amdgcn_permlane16_swap(__float_as_uint(acc), __float_as_uint(acc), false, false);
      acc = __uint_as_float(y[0]) + __uint_as_float(y[1]);
      float deep = rbf(rbf(acc) + b2v);
      float z = rbf(wide + deep);
      float o = 1.f / (1.f + __expf(-z));
      if (lane < 16) out[m0 + 16 * c8 + lane] = __builtin_bit_cast(unsigned short, __float2bfloat16(o));
      if (c8 == 0 && lane == 0) __builtin_amdgcn_raw_buffer_store_b32(e0, rsS, 0, (int)(WS_EPOCH + mt * 128), 16);
      ST(8);
    }
  }
}

__global__ void report(const char* ws) {
  const unsigned long long* st = (const unsigned long long*)(ws + (16 << 20));
  int i = threadIdx.x;
  __shared__ unsigned long long a[256][10];
  for (int k = 0; k < 10; ++k) a[i][k] = st[i * 40 + k];
  __syncthreads();
  if (i == 0) {
    unsigned long long mn = ~0ull;
    for (int j = 0; j < 256; ++j) mn = a[j][0] < mn ? a[j][0] : mn;
    unsigned long long mx[10], sum[10];
    for (int k = 0; k < 10; ++k) { mx[k] = 0; sum[k] = 0; for (int j = 0; j < 256; ++j) { unsigned long long v = a[j][k] - mn; mx[k] = v > mx[k] ? v : mx[k]; sum[k] += v; } }
    unsigned long long t0 = __builtin_amdgcn_s_memrealtime();
    unsigned long long m256 = mx[0]*256;
    if (m256 > 5000ull * 256) m256 = 5000ull * 256;
    while (__builtin_amdgcn_s_memrealtime() - t0 < (m256 * 10) / 256) __builtin_amdgcn_s_sleep(1);
  }
}
extern "C" void kernel_launch(void* const* d_in, const int* in_sizes, int n_in, void* d_out, int out_size, void* d_ws,
                              size_t ws_size, hipStream_t stream) {
  const int* idx = (const int*)d_in[0];
  const unsigned short* table = (const unsigned short*)d_in[1];
  const unsigned short* xw = (const unsigned short*)d_in[2];
  const unsigned short* ww = (const unsigned short*)d_in[3];
  const unsigned short* bw = (const unsigned short*)d_in[4];
  const unsigned short* W1 = (const unsigned short*)d_in[5];
  const unsigned short* b1 = (const unsigned short*)d_in[6];
  const unsigned short* W2 = (const unsigned short*)d_in[7];
  const unsigned short* b2 = (const unsigned short*)d_in[8];
  int V = in_sizes[1] / D;
  (void)hipFuncSetAttribute((const void*)k1, hipFuncAttributeMaxDynamicSharedMemorySize, LDS_BYTES);
  k1<<<256, 512, LDS_BYTES, stream>>>(idx, table, xw, ww, bw, W1, b1, W2, b2, (unsigned short*)d_out, (char*)d_ws, V);
  report<<<1, 256, 0, stream>>>((const char*)d_ws);
}
```

```cpp
#define STAMP
#include <hip/hip_runtime.h>
#include <hip/hip_bf16.h>
#include <stdint.h>

typedef __attribute__((ext_vector_type(8))) short bf16x8;
typedef __attribute__((ext_vector_type(4))) short bf16x4;
typedef __attribute__((ext_vector_type(2))) short s16x2;
typedef __attribute__((ext_vector_type(2))) __bf16 b16x2;
typedef __attribute__((ext_vector_type(2))) float f32x2;
typedef __attribute__((ext_vector_type(4))) float f32x4;
typedef __attribute__((ext_vector_type(32))) float f32x32;
typedef __attribute__((ext_vector_type(4))) unsigned u32x4;
typedef __attribute__((ext_vector_type(2))) unsigned u32x2;
typedef __attribute__((ext_vector_type(4))) int i32x4;

#define NP1 11
#define SCHED 0
#define DR 2
#define PL 3
#define XROT 3

constexpr int Bsz = 4096, F = 26, D = 128, H = 512, ND = 13;
constexpr int NP2 = F - 2 * NP1, NS = 2 * (NP1 + NP2), NS1 = 2 * NP1;
constexpr int SLOT = 32768, NSLOT = 4, IDXOFF = SLOT * NSLOT, LDS_BYTES = IDXOFF + 13312 + 768;
constexpr unsigned WS_EPOCH = 0, WS_FLAG = 4096, WS_GRAN = 65536, WS_PAY = 2u << 20, WS_END = WS_PAY + 256u * 32768u;
constexpr int SPIN_MAX = 1 << 16;

__device__ __forceinline__ float bf2f(unsigned short x) { return __uint_as_float(((unsigned)x) << 16); }
__device__ __forceinline__ float rbf(float x) { return __bfloat162float(__float2bfloat16(x)); }

__device__ __forceinline__ i32x4 mk_rsrc(const void* p, unsigned bytes) {
  unsigned long long a = (unsigned long long)p;
  i32x4 r;
  r[0] = __builtin_amdgcn_readfirstlane((int)(unsigned)a);
  r[1] = __builtin_amdgcn_readfirstlane((int)((unsigned)(a >> 32) & 0xffff));
  r[2] = __builtin_amdgcn_readfirstlane((int)bytes);
  r[3] = 0x00020000;
  return r;
}
__device__ __forceinline__ void bdma(unsigned ldsbase, unsigned voff, i32x4 rs, unsigned soff) {
  asm volatile("s_mov_b32 m0, %0\n\ts_nop 0\n\tbuffer_load_dwordx4 %1, %2, %3 offen lds" ::"s"(ldsbase), "v"(voff), "s"(rs), "s"(soff) : "memory");
}
template <int N> __device__ __forceinline__ void waitv() { asm volatile("s_waitcnt vmcnt(%0)" ::"i"(N) : "memory"); }
__device__ __forceinline__ void bar() { asm volatile("s_barrier" ::: "memory"); }
__device__ __forceinline__ void bar_lgkm() { asm volatile("s_waitcnt lgkmcnt(0)\n\ts_barrier" ::: "memory"); }

typedef __attribute__((address_space(3))) char lds_char;
typedef __attribute__((address_space(3))) bf16x8 lds_bf16x8;
typedef __attribute__((address_space(3))) bf16x4 lds_bf16x4;
typedef __attribute__((address_space(3))) int lds_int;

#define ST(k) do { unsigned long long _a = __builtin_amdgcn_s_memrealtime(), _b = __builtin_amdgcn_s_memtime(); if (lane == 0) { ((unsigned long long*)(ws + (16 << 20)))[bid * 40 + (k)] = _a; ((unsigned long long*)(ws + (17 << 20)))[bid * 40 + (k)] = _b; } } while (0)

__device__ __forceinline__ int sched(int k, int r) {
  if (k == 0) return r;
  const int tstar = NS - 4 + ((r - NS) & 3);
  int t = k - 1;
  t += (t >= tstar) ? 1 : 0;
  return ((t & 3) == r) ? t + 4 : t;
}

__global__ __launch_bounds__(512, 2) void k1(const int* __restrict__ idx, const unsigned short* __restrict__ table,
                                             const unsigned short* __restrict__ xw, const unsigned short* __restrict__ ww,
                                             const unsigned short* __restrict__ bw, const unsigned short* __restrict__ W1,
                                             const unsigned short* __restrict__ b1, const unsigned short* __restrict__ W2,
                                             const unsigned short* __restrict__ b2, unsigned short* __restrict__ out,
                                             char* ws, int V) {
  extern __shared__ __attribute__((aligned(16))) char smem[];
  lds_char* lds = (lds_char*)smem;
  const unsigned lds0 = (unsigned)(uintptr_t)lds;
  const int tid = threadIdx.x, lane = tid & 63;
  const int w = __builtin_amdgcn_readfirstlane(tid >> 6);
  const int bid = blockIdx.x;
  const int xcd = bid & 7, slot = bid >> 3;
  const int ml = slot >> 3, c8 = slot & 7;
  const int nt = c8 >> 1, p = c8 & 1;
  const int mt = xcd * 4 + ml;
  const int m0 = mt * 128;
  const int nown = nt * 128 + p * 64, noth = nt * 128 + (1 - p) * 64;
  const int r = (ml - nt) & 3;

  if (w >= 4) {
    const int lw = w - 4;
    const int g = lane >> 3, c = lane & 7;
    i32x4 rsI = mk_rsrc(idx, Bsz * F * 4);
    i32x4 rsT = mk_rsrc(table, (unsigned)V * D * 2);
    i32x4 rsW = mk_rsrc(W1, F * D * H * 2);
    asm volatile("s_nop 4");
    {
      unsigned vo = (unsigned)(lane * 16);
      unsigned so = (unsigned)((m0 + lw * 32) * F * 4);
#pragma unroll
      for (int j = 0; j < 4; ++j) bdma(lds0 + IDXOFF + lw * 3328 + j * 1024, vo, rsI, so + j * 1024);
    }
    const int rot = XROT * xcd;
    const unsigned bo0 = (unsigned)((16 * lw + g) * H * 2 + ((c ^ (((g >> 1) & 1) << 1)) << 4));
    const unsigned bo1 = (unsigned)((16 * lw + 8 + g) * H * 2 + ((c ^ ((((g >> 1) & 1) | 2) << 1)) << 4));
    const unsigned sw0 = (unsigned)((c ^ (g >> 1)) << 4), sw1 = (unsigned)((c ^ (4 + (g >> 1))) << 4);
    auto field = [&](int u) {
      int i = u >> 1;
      int f = (i < NP1 ? p * NP1 + i : NP1 + i) + rot;
      f = f >= F ? f - F : f;
      return f;
    };
    auto issueB = [&](int u, int f, unsigned sb) {
      const unsigned so = (unsigned)((f * 128 + (u & 1) * 64) * H * 2);
      bdma(sb + lw * 2048, bo0, rsW, so + nown * 2);
      bdma(sb + lw * 2048 + 1024, bo1, rsW, so + nown * 2);
      if (u < NS1) {
        bdma(sb + 8192 + lw * 2048, bo0, rsW, so + noth * 2);
        bdma(sb + 8192 + lw * 2048 + 1024, bo1, rsW, so + noth * 2);
      }
    };
    const lds_int* sidx = (const lds_int*)(lds + IDXOFF) + (lw * 32 + g) * F;
    auto getoff = [&](int f, unsigned (&vo)[4]) {
#pragma unroll
      for (int j = 0; j < 4; ++j) {
        int ix = sidx[j * 8 * F + f];
        ix += (ix >> 31) & V;
        vo[j] = ((unsigned)ix << 8) | ((j & 1) ? sw1 : sw0);
      }
    };
    auto issueA = [&](int u, unsigned (&vo)[4], unsigned sb) {
      const unsigned so = (unsigned)((u & 1) * 128);
#pragma unroll
      for (int j = 0; j < 4; ++j) bdma(sb + 16384 + lw * 4096 + j * 1024, vo[j], rsT, so);
    };
    const int u0 = sched(0, r), u1 = sched(1, r), u2 = sched(2, r);
    const int f0 = field(u0), f1 = field(u1), f2 = field(u2);
    issueB(u0, f0, lds0); issueB(u1, f1, lds0 + SLOT); issueB(u2, f2, lds0 + 2 * SLOT);
    waitv<12>();
    unsigned vo[4], v1[4], v2[4];
    getoff(f0, vo); getoff(f1, v1); getoff(f2, v2);
    issueA(u0, vo, lds0); issueA(u1, v1, lds0 + SLOT); issueA(u2, v2, lds0 + 2 * SLOT);
    int un = sched(3, r), fn = field(un);
    getoff(fn, vo);
    waitv<8>();
    unsigned sb = lds0 + 3 * SLOT;
    for (int k = 0; k < NS - 3; ++k) {
      bar();
      issueB(un, fn, sb); issueA(un, vo, sb);
      sb += SLOT; if (sb == lds0 + NSLOT * SLOT) sb = lds0;
      un = sched(k + 4 < NS ? k + 4 : NS - 1, r); fn = field(un);
      getoff(fn, vo);
      if (k == 0) waitv<10>(); else waitv<12>();
    }
    bar();
    waitv<6>(); bar();
    waitv<0>(); bar();
  } else {
    const int nq = w & 1, mw = w >> 1;
    const int G = lane >> 4, q = (lane >> 2) & 3, pp = lane & 3, c16 = lane & 15;
    if (w == 0) ST(0);
    const int gid = mt * 8 + c8;
    __amdgpu_buffer_rsrc_t rsS = __builtin_amdgcn_make_buffer_rsrc((void*)ws, 0, (int)WS_END, 0x00020000);
    u32x2 ep = __builtin_bit_cast(u32x2, __builtin_amdgcn_raw_buffer_load_b64(rsS, 0, (int)(WS_EPOCH + mt * 128), 16));
    u32x2 pb[2], pw[2];
#pragma unroll
    for (int b = 0; b < 2; ++b) {
      int n = nown + 32 * nq + 16 * b + 4 * G;
      pb[b] = *(const u32x2*)(b1 + n);
      pw[b] = *(const u32x2*)(W2 + n);
    }
    float wide;
    {
      const int smp = m0 + 16 * c8 + c16;
      float wd = 0.f;
#pragma unroll
      for (int j = 0; j < ND; ++j) wd += bf2f(xw[smp * ND + j]) * bf2f(ww[j]);
      wide = rbf(rbf(wd) + bf2f(bw[0]));
    }
    float b2v = bf2f(b2[0]);
    asm volatile("" : "+v"(wide), "+v"(b2v));
    const unsigned e0 = __builtin_amdgcn_readfirstlane(ep[0]) + 1u, e1 = __builtin_amdgcn_readfirstlane(ep[1]);
    const unsigned tag0 = e0 * 0x9E3779B1u + 0x7F4A7C15u, tag1 = (e0 ^ 0x5bd1e995u) * 0x85EBCA6Bu + e1;
    int e = 0;
    unsigned mask = 0;
    for (int k = 0; k < NS; ++k) { const bool t = sched(k, r) < NS1; e = t ? k : e; mask |= t ? (1u << k) : 0u; }

    unsigned wa[2], ea[2];
    {
      const int key2 = (q >> 1) | ((G & 1) << 1);
#pragma unroll
      for (int b = 0; b < 2; ++b) wa[b] = lds0 + (unsigned)((8 * G + q) * 128 + (((2 * (2 * nq + b) + (pp >> 1)) ^ (key2 << 1)) << 4) + 8 * (pp & 1));
#pragma unroll
      for (int g = 0; g < 2; ++g) ea[g] = lds0 + (unsigned)(16384 + (64 * mw + c16) * 128 + (((4 * g + G) ^ (c16 >> 1)) << 4));
    }
    f32x32 own, oth;
#pragma unroll
    for (int b = 0; b < 2; ++b) {
      const unsigned p0 = pb[b][0], p1 = pb[b][1];
#pragma unroll
      for (int m = 0; m < 4; ++m) {
        own[(b * 4 + m) * 4 + 0] = __uint_as_float(p0 << 16);
        own[(b * 4 + m) * 4 + 1] = __uint_as_float(p0 & 0xffff0000u);
        own[(b * 4 + m) * 4 + 2] = __uint_as_float(p1 << 16);
        own[(b * 4 + m) * 4 + 3] = __uint_as_float(p1 & 0xffff0000u);
      }
    }
    {
      const i32x4 srd = mk_rsrc(ws, WS_END);
      const unsigned pos = WS_PAY + (unsigned)gid * 32768u + (unsigned)w * 8192u;
      const unsigned pop = WS_PAY + (unsigned)(gid ^ 1) * 32768u + (unsigned)w * 8192u;
      const unsigned mfl = WS_FLAG + (unsigned)gid * 128u, pfl = WS_FLAG + (unsigned)(gid ^ 1) * 128u;
      const int kd = e + DR, kdw = (w == 0) ? kd : -1, kp = NS - PL;
      unsigned sk, sso, st0, st1, scnt;
      unsigned long long q0, q1, q2, q3, q4;
#define STAMPASM(i) "s_memrealtime %[q" #i "]\n\ts_waitcnt lgkmcnt(0)\n\t"
      asm volatile(
      "s_nop 4\n\t"
      "v_mov_b32 v96, 0\n\t"
      "v_mov_b32 v97, 0\n\t"
      "v_mov_b32 v98, 0\n\t"
      "v_mov_b32 v99, 0\n\t"
      "v_mov_b32 v100, 0\n\t"
      "v_mov_b32 v101, 0\n\t"
      "v_mov_b32 v102, 0\n\t"
      "v_mov_b32 v103, 0\n\t"
      "v_mov_b32 v104, 0\n\t"
      "v_mov_b32 v105, 0\n\t"
      "v_mov_b32 v106, 0\n\t"
      "v_mov_b32 v107, 0\n\t"
      "v_mov_b32 v108, 0\n\t"
      "v_mov_b32 v109, 0\n\t"
      "v_mov_b32 v110, 0\n\t"
      "v_mov_b32 v111, 0\n\t"
      "v_mov_b32 v112, 0\n\t"
      "v_mov_b32 v113, 0\n\t"
      "v_mov_b32 v114, 0\n\t"
      "v_mov_b32 v115, 0\n\t"
      "v_mov_b32 v116, 0\n\t"
      "v_mov_b32 v117, 0\n\t"
      "v_mov_b32 v118, 0\n\t"
      "v_mov_b32 v119, 0\n\t"
      "v_mov_b32 v120, 0\n\t"
      "v_mov_b32 v121, 0\n\t"
      "v_mov_b32 v122, 0\n\t"
      "v_mov_b32 v123, 0\n\t"
      "v_mov_b32 v124, 0\n\t"
      "v_mov_b32 v125, 0\n\t"
      "v_mov_b32 v126, 0\n\t"
      "v_mov_b32 v127, 0\n\t"
      "v_mov_b32 v192, %[wa0]\n\t"
      "v_mov_b32 v193, %[wa1]\n\t"
      "v_mov_b32 v194, %[ea0]\n\t"
      "v_mov_b32 v195, %[ea1]\n\t"
      "v_mov_b32 v198, %[tagA]\n\t"
      "v_mov_b32 v199, %[tagB]\n\t"
      "s_mov_b32 %[k], 0\n\t"
      "s_mov_b32 %[so], 0\n\t"
      "s_waitcnt vmcnt(0) lgkmcnt(0)\n\t"
      "s_barrier\n\t"
      STAMPASM(0)
      "ds_read_b64_tr_b16 v[128:129], v192 offset:0\n\t"
      "ds_read_b64_tr_b16 v[130:131], v192 offset:512\n\t"
      "ds_read_b128 v[144:147], v194 offset:0\n\t"
      "ds_read_b128 v[148:151], v194 offset:2048\n\t"
      "ds_read_b128 v[152:155], v194 offset:4096\n\t"
      "ds_read_b128 v[156:159], v194 offset:6144\n\t"
      "ds_read_b64_tr_b16 v[132:133], v193 offset:0\n\t"
      "ds_read_b64_tr_b16 v[134:135], v193 offset:512\n\t"
      "ds_read_b64_tr_b16 v[136:137], v192 offset:8192\n\t"
      "ds_read_b64_tr_b16 v[138:139], v192 offset:8704\n\t"
      "ds_read_b64_tr_b16 v[140:141], v193 offset:8192\n\t"
      "ds_read_b64_tr_b16 v[142:143], v193 offset:8704\n\t"
      "L1_%=:\n\t"
      "s_waitcnt lgkmcnt(0)\n\t"
      "v_mfma_f32_16x16x32_bf16 v[64:67], v[128:131], v[144:147], v[64:67]\n\t"
      "ds_read_b64_tr_b16 v[160:161], v192 offset:4096\n\t"
      "ds_read_b64_tr_b16 v[162:163], v192 offset:4608\n\t"
      "v_mfma_f32_16x16x32_bf16 v[68:71], v[128:131], v[148:151], v[68:71]\n\t"
      "ds_read_b128 v[176:179], v195 offset:0\n\t"
      "v_mfma_f32_16x16x32_bf16 v[72:75], v[128:131], v[152:155], v[72:75]\n\t"
      "ds_read_b128 v[180:183], v195 offset:2048\n\t"
      "v_mfma_f32_16x16x32_bf16 v[76:79], v[128:131], v[156:159], v[76:79]\n\t"
      "ds_read_b128 v[184:187], v195 offset:4096\n\t"
      "v_mfma_f32_16x16x32_bf16 v[80:83], v[132:135], v[144:147], v[80:83]\n\t"
      "ds_read_b128 v[188:191], v195 offset:6144\n\t"
      "v_mfma_f32_16x16x32_bf16 v[84:87], v[132:135], v[148:151], v[84:87]\n\t"
      "ds_read_b64_tr_b16 v[164:165], v193 offset:4096\n\t"
      "ds_read_b64_tr_b16 v[166:167], v193 offset:4608\n\t"
      "v_mfma_f32_16x16x32_bf16 v[88:91], v[132:135], v[152:155], v[88:91]\n\t"
      "ds_read_b64_tr_b16 v[168:169], v192 offset:12288\n\t"
      "ds_read_b64_tr_b16 v[170:171], v192 offset:12800\n\t"
      "v_mfma_f32_16x16x32_bf16 v[92:95], v[132:135], v[156:159], v[92:95]\n\t"
      "ds_read_b64_tr_b16 v[172:173], v193 offset:12288\n\t"
      "ds_read_b64_tr_b16 v[174:175], v193 offset:12800\n\t"
      "v_mfma_f32_16x16x32_bf16 v[96:99], v[136:139], v[144:147], v[96:99]\n\t"
      "s_add_u32 %[so], %[so], 0x8000\n\t"
      "s_and_b32 %[so], %[so], 0x1ffff\n\t"
      "v_mfma_f32_16x16x32_bf16 v[100:103], v[136:139], v[148:151], v[100:103]\n\t"
      "v_add_u32 v192, %[so], %[wa0]\n\t"
      "v_add_u32 v193, %[so], %[wa1]\n\t"
      "v_mfma_f32_16x16x32_bf16 v[104:107], v[136:139], v[152:155], v[104:107]\n\t"
      "v_add_u32 v194, %[so], %[ea0]\n\t"
      "v_add_u32 v195, %[so], %[ea1]\n\t"
      "v_mfma_f32_16x16x32_bf16 v[108:111], v[136:139], v[156:159], v[108:111]\n\t"
      "v_mfma_f32_16x16x32_bf16 v[112:115], v[140:143], v[144:147], v[112:115]\n\t"
      "v_mfma_f32_16x16x32_bf16 v[116:119], v[140:143], v[148:151], v[116:119]\n\t"
      "v_mfma_f32_16x16x32_bf16 v[120:123], v[140:143], v[152:155], v[120:123]\n\t"
      "v_mfma_f32_16x16x32_bf16 v[124:127], v[140:143], v[156:159], v[124:127]\n\t"
      "s_waitcnt lgkmcnt(0)\n\t"
      "s_barrier\n\t"
      "v_mfma_f32_16x16x32_bf16 v[64:67], v[160:163], v[176:179], v[64:67]\n\t"
      "ds_read_b64_tr_b16 v[128:129], v192 offset:0\n\t"
      "ds_read_b64_tr_b16 v[130:131], v192 offset:512\n\t"
      "v_mfma_f32_16x16x32_bf16 v[68:71], v[160:163], v[180:183], v[68:71]\n\t"
      "ds_read_b128 v[144:147], v194 offset:0\n\t"
      "v_mfma_f32_16x16x32_bf16 v[72:75], v[160:163], v[184:187], v[72:75]\n\t"
      "ds_read_b128 v[148:151], v194 offset:2048\n\t"
      "v_mfma_f32_16x16x32_bf16 v[76:79], v[160:163], v[188:191], v[76:79]\n\t"
      "ds_read_b128 v[152:155], v194 offset:4096\n\t"
      "v_mfma_f32_16x16x32_bf16 v[80:83], v[164:167], v[176:179], v[80:83]\n\t"
      "ds_read_b128 v[156:159], v194 offset:6144\n\t"
      "v_mfma_f32_16x16x32_bf16 v[84:87], v[164:167], v[180:183], v[84:87]\n\t"
      "ds_read_b64_tr_b16 v[132:133], v193 offset:0\n\t"
      "ds_read_b64_tr_b16 v[134:135], v193 offset:512\n\t"
      "v_mfma_f32_16x16x32_bf16 v[88:91], v[164:167], v[184:187], v[88:91]\n\t"
      "ds_read_b64_tr_b16 v[136:137], v192 offset:8192\n\t"
      "ds_read_b64_tr_b16 v[138:139], v192 offset:8704\n\t"
      "v_mfma_f32_16x16x32_bf16 v[92:95], v[164:167], v[188:191], v[92:95]\n\t"
      "ds_read_b64_tr_b16 v[140:141], v193 offset:8192\n\t"
      "ds_read_b64_tr_b16 v[142:143], v193 offset:8704\n\t"
      "v_mfma_f32_16x16x32_bf16 v[96:99], v[168:171], v[176:179], v[96:99]\n\t"
      "s_add_u32 %[k], %[k], 1\n\t"
      "v_mfma_f32_16x16x32_bf16 v[100:103], v[168:171], v[180:183], v[100:103]\n\t"
      "s_bitcmp1_b32 %[mask], %[k]\n\t"
      "v_mfma_f32_16x16x32_bf16 v[104:107], v[168:171], v[184:187], v[104:107]\n\t"
      "v_mfma_f32_16x16x32_bf16 v[108:111], v[168:171], v[188:191], v[108:111]\n\t"
      "v_mfma_f32_16x16x32_bf16 v[112:115], v[172:175], v[176:179], v[112:115]\n\t"
      "v_mfma_f32_16x16x32_bf16 v[116:119], v[172:175], v[180:183], v[116:119]\n\t"
      "v_mfma_f32_16x16x32_bf16 v[120:123], v[172:175], v[184:187], v[120:123]\n\t"
      "v_mfma_f32_16x16x32_bf16 v[124:127], v[172:175], v[188:191], v[124:127]\n\t"
      "s_cbranch_scc1 L1_%=\n\t"
      "L2_%=:\n\t"
      "s_cmp_gt_u32 %[k], %[e]\n\t"
      "s_cbranch_scc1 L3_%=\n\t"
      "s_waitcnt lgkmcnt(0)\n\t"
      "v_mfma_f32_16x16x32_bf16 v[64:67], v[128:131], v[144:147], v[64:67]\n\t"
      "ds_read_b64_tr_b16 v[160:161], v192 offset:4096\n\t"
      "ds_read_b64_tr_b16 v[162:163], v192 offset:4608\n\t"
      "v_mfma_f32_16x16x32_bf16 v[68:71], v[128:131], v[148:151], v[68:71]\n\t"
      "ds_read_b128 v[176:179], v195 offset:0\n\t"
      "v_mfma_f32_16x16x32_bf16 v[72:75], v[128:131], v[152:155], v[72:75]\n\t"
      "ds_read_b128 v[180:183], v195 offset:2048\n\t"
      "v_mfma_f32_16x16x32_bf16 v[76:79], v[128:131], v[156:159], v[76:79]\n\t"
      "ds_read_b128 v[184:187], v195 offset:4096\n\t"
      "v_mfma_f32_16x16x32_bf16 v[80:83], v[132:135], v[144:147], v[80:83]\n\t"
      "ds_read_b128 v[188:191], v195 offset:6144\n\t"
      "v_mfma_f32_16x16x32_bf16 v[84:87], v[132:135], v[148:151], v[84:87]\n\t"
      "ds_read_b64_tr_b16 v[164:165], v193 offset:4096\n\t"
      "ds_read_b64_tr_b16 v[166:167], v193 offset:4608\n\t"
      "v_mfma_f32_16x16x32_bf16 v[88:91], v[132:135], v[152:155], v[88:91]\n\t"
      "s_add_u32 %[so], %[so], 0x8000\n\t"
      "s_and_b32 %[so], %[so], 0x1ffff\n\t"
      "v_add_u32 v192, %[so], %[wa0]\n\t"
      "v_add_u32 v193, %[so], %[wa1]\n\t"
      "v_mfma_f32_16x16x32_bf16 v[92:95], v[132:135], v[156:159], v[92:95]\n\t"
      "v_add_u32 v194, %[so], %[ea0]\n\t"
      "v_add_u32 v195, %[so], %[ea1]\n\t"
      "s_waitcnt lgkmcnt(0)\n\t"
      "s_barrier\n\t"
      "v_mfma_f32_16x16x32_bf16 v[64:67], v[160:163], v[176:179], v[64:67]\n\t"
      "ds_read_b64_tr_b16 v[128:129], v192 offset:0\n\t"
      "ds_read_b64_tr_b16 v[130:131], v192 offset:512\n\t"
      "v_mfma_f32_16x16x32_bf16 v[68:71], v[160:163], v[180:183], v[68:71]\n\t"
      "ds_read_b128 v[144:147], v194 offset:0\n\t"
      "v_mfma_f32_16x16x32_bf16 v[72:75], v[160:163], v[184:187], v[72:75]\n\t"
      "ds_read_b128 v[148:151], v194 offset:2048\n\t"
      "v_mfma_f32_16x16x32_bf16 v[76:79], v[160:163], v[188:191], v[76:79]\n\t"
      "ds_read_b128 v[152:155], v194 offset:4096\n\t"
      "v_mfma_f32_16x16x32_bf16 v[80:83], v[164:167], v[176:179], v[80:83]\n\t"
      "ds_read_b128 v[156:159], v194 offset:6144\n\t"
      "v_mfma_f32_16x16x32_bf16 v[84:87], v[164:167], v[180:183], v[84:87]\n\t"
      "ds_read_b64_tr_b16 v[132:133], v193 offset:0\n\t"
      "ds_read_b64_tr_b16 v[134:135], v193 offset:512\n\t"
      "v_mfma_f32_16x16x32_bf16 v[88:91], v[164:167], v[184:187], v[88:91]\n\t"
      "ds_read_b64_tr_b16 v[136:137], v192 offset:8192\n\t"
      "ds_read_b64_tr_b16 v[138:139], v192 offset:8704\n\t"
      "v_mfma_f32_16x16x32_bf16 v[92:95], v[164:167], v[188:191], v[92:95]\n\t"
      "ds_read_b64_tr_b16 v[140:141], v193 offset:8192\n\t"
      "ds_read_b64_tr_b16 v[142:143], v193 offset:8704\n\t"
      "s_add_u32 %[k], %[k], 1\n\t"
      "s_bitcmp1_b32 %[mask], %[k]\n\t"
      "s_cbranch_scc1 L1_%=\n\t"
      "s_branch L2_%=\n\t"
      "L3_%=:\n\t"
      STAMPASM(1)
      "s_add_u32 %[t0], %[pos], 0x1000\n\t"
      "s_nop 7\n\t"
      "buffer_store_dwordx4 v[96:99], %[l16], %[srd], %[pos] offen offset:0 sc1\n\t"
      "buffer_store_dwordx4 v[100:103], %[l16], %[srd], %[pos] offen offset:1024 sc1\n\t"
      "buffer_store_dwordx4 v[104:107], %[l16], %[srd], %[pos] offen offset:2048 sc1\n\t"
      "buffer_store_dwordx4 v[108:111], %[l16], %[srd], %[pos] offen offset:3072 sc1\n\t"
      "buffer_store_dwordx4 v[112:115], %[l16], %[srd], %[t0] offen offset:0 sc1\n\t"
      "buffer_store_dwordx4 v[116:119], %[l16], %[srd], %[t0] offen offset:1024 sc1\n\t"
      "buffer_store_dwordx4 v[120:123], %[l16], %[srd], %[t0] offen offset:2048 sc1\n\t"
      "buffer_store_dwordx4 v[124:127], %[l16], %[srd], %[t0] offen offset:3072 sc1\n\t"
      "L4_%=:\n\t"
      "s_cmp_lg_u32 %[k], %[kp]\n\t"
      "s_cbranch_scc1 L6_%=\n\t"
      STAMPASM(2)
      "s_mov_b32 %[cnt], 0x10000\n\t"
      "L5_%=:\n\t"
      "buffer_load_dwordx2 v[196:197], off, %[srd], %[pfl] sc1\n\t"
      "s_sub_u32 %[cnt], %[cnt], 1\n\t"
      "s_cbranch_scc1 L7_%=\n\t"
      "s_waitcnt vmcnt(0)\n\t"
      "v_readfirstlane_b32 %[t0], v196\n\t"
      "v_readfirstlane_b32 %[t1], v197\n\t"
      "s_nop 1\n\t"
      "s_xor_b32 %[t0], %[t0], %[tagA]\n\t"
      "s_xor_b32 %[t1], %[t1], %[tagB]\n\t"
      "s_or_b32 %[t0], %[t0], %[t1]\n\t"
      "s_cbranch_scc1 L5_%=\n\t"
      "L7_%=:\n\t"
      STAMPASM(3)
      "s_add_u32 %[t0], %[pop], 0x1000\n\t"
      "buffer_load_dwordx4 v[96:99], %[l16], %[srd], %[pop] offen offset:0 sc1\n\t"
      "buffer_load_dwordx4 v[100:103], %[l16], %[srd], %[pop] offen offset:1024 sc1\n\t"
      "buffer_load_dwordx4 v[104:107], %[l16], %[srd], %[pop] offen offset:2048 sc1\n\t"
      "buffer_load_dwordx4 v[108:111], %[l16], %[srd], %[pop] offen offset:3072 sc1\n\t"
      "buffer_load_dwordx4 v[112:115], %[l16], %[srd], %[t0] offen offset:0 sc1\n\t"
      "buffer_load_dwordx4 v[116:119], %[l16], %[srd], %[t0] offen offset:1024 sc1\n\t"
      "buffer_load_dwordx4 v[120:123], %[l16], %[srd], %[t0] offen offset:2048 sc1\n\t"
      "buffer_load_dwordx4 v[124:127], %[l16], %[srd], %[t0] offen offset:3072 sc1\n\t"
      "L6_%=:\n\t"
      "s_waitcnt lgkmcnt(5)\n\t"
      "v_mfma_f32_16x16x32_bf16 v[64:67], v[128:131], v[144:147], v[64:67]\n\t"
      "ds_read_b64_tr_b16 v[160:161], v192 offset:4096\n\t"
      "ds_read_b64_tr_b16 v[162:163], v192 offset:4608\n\t"
      "s_waitcnt lgkmcnt(6)\n\t"
      "v_mfma_f32_16x16x32_bf16 v[68:71], v[128:131], v[148:151], v[68:71]\n\t"
      "ds_read_b128 v[176:179], v195 offset:0\n\t"
      "s_waitcnt lgkmcnt(6)\n\t"
      "v_mfma_f32_16x16x32_bf16 v[72:75], v[128:131], v[152:155], v[72:75]\n\t"
      "ds_read_b128 v[180:183], v195 offset:2048\n\t"
      "s_waitcnt lgkmcnt(6)\n\t"
      "v_mfma_f32_16x16x32_bf16 v[76:79], v[128:131], v[156:159], v[76:79]\n\t"
      "ds_read_b128 v[184:187], v195 offset:4096\n\t"
      "s_waitcnt lgkmcnt(5)\n\t"
      "v_mfma_f32_16x16x32_bf16 v[80:83], v[132:135], v[144:147], v[80:83]\n\t"
      "ds_read_b128 v[188:191], v195 offset:6144\n\t"
      "v_mfma_f32_16x16x32_bf16 v[84:87], v[132:135], v[148:151], v[84:87]\n\t"
      "ds_read_b64_tr_b16 v[164:165], v193 offset:4096\n\t"
      "ds_read_b64_tr_b16 v[166:167], v193 offset:4608\n\t"
      "v_mfma_f32_16x16x32_bf16 v[88:91], v[132:135], v[152:155], v[88:91]\n\t"
      "s_add_u32 %[so], %[so], 0x8000\n\t"
      "s_and_b32 %[so], %[so], 0x1ffff\n\t"
      "v_add_u32 v192, %[so], %[wa0]\n\t"
      "v_add_u32 v193, %[so], %[wa1]\n\t"
      "v_mfma_f32_16x16x32_bf16 v[92:95], v[132:135], v[156:159], v[92:95]\n\t"
      "v_add_u32 v194, %[so], %[ea0]\n\t"
      "v_add_u32 v195, %[so], %[ea1]\n\t"
      "s_cmp_eq_u32 %[k], %[nsm1]\n\t"
      "s_cbranch_scc1 L9_%=\n\t"
      "s_cmp_lg_u32 %[k], %[kd]\n\t"
      "s_cbranch_scc1 L8_%=\n\t"
      "s_waitcnt vmcnt(0)\n\t"
      "L8_%=:\n\t"
      "s_waitcnt lgkmcnt(0)\n\t"
      "s_barrier\n\t"
      "s_cmp_lg_u32 %[k], %[kdw]\n\t"
      "s_cbranch_scc1 L10_%=\n\t"
      "s_mov_b64 exec, 1\n\t"
      "buffer_store_dwordx2 v[198:199], off, %[srd], %[mfl] sc1\n\t"
      "s_mov_b64 exec, -1\n\t"
      "L10_%=:\n\t"
      "v_mfma_f32_16x16x32_bf16 v[64:67], v[160:163], v[176:179], v[64:67]\n\t"
      "ds_read_b64_tr_b16 v[128:129], v192 offset:0\n\t"
      "ds_read_b64_tr_b16 v[130:131], v192 offset:512\n\t"
      "v_mfma_f32_16x16x32_bf16 v[68:71], v[160:163], v[180:183], v[68:71]\n\t"
      "ds_read_b128 v[144:147], v194 offset:0\n\t"
      "v_mfma_f32_16x16x32_bf16 v[72:75], v[160:163], v[184:187], v[72:75]\n\t"
      "ds_read_b128 v[148:151], v194 offset:2048\n\t"
      "v_mfma_f32_16x16x32_bf16 v[76:79], v[160:163], v[188:191], v[76:79]\n\t"
      "ds_read_b128 v[152:155], v194 offset:4096\n\t"
      "v_mfma_f32_16x16x32_bf16 v[80:83], v[164:167], v[176:179], v[80:83]\n\t"
      "ds_read_b128 v[156:159], v194 offset:6144\n\t"
      "v_mfma_f32_16x16x32_bf16 v[84:87], v[164:167], v[180:183], v[84:87]\n\t"
      "ds_read_b64_tr_b16 v[132:133], v193 offset:0\n\t"
      "ds_read_b64_tr_b16 v[134:135], v193 offset:512\n\t"
      "v_mfma_f32_16x16x32_bf16 v[88:91], v[164:167], v[184:187], v[88:91]\n\t"
      "s_add_u32 %[k], %[k], 1\n\t"
      "v_mfma_f32_16x16x32_bf16 v[92:95], v[164:167], v[188:191], v[92:95]\n\t"
      "s_branch L4_%=\n\t"
      "L9_%=:\n\t"
      "s_waitcnt lgkmcnt(5)\n\t"
      "v_mfma_f32_16x16x32_bf16 v[64:67], v[160:163], v[176:179], v[64:67]\n\t"
      "s_waitcnt lgkmcnt(4)\n\t"
      "v_mfma_f32_16x16x32_bf16 v[68:71], v[160:163], v[180:183], v[68:71]\n\t"
      "s_waitcnt lgkmcnt(3)\n\t"
      "v_mfma_f32_16x16x32_bf16 v[72:75], v[160:163], v[184:187], v[72:75]\n\t"
      "s_waitcnt lgkmcnt(2)\n\t"
      "v_mfma_f32_16x16x32_bf16 v[76:79], v[160:163], v[188:191], v[76:79]\n\t"
      "s_waitcnt lgkmcnt(0)\n\t"
      "v_mfma_f32_16x16x32_bf16 v[80:83], v[164:167], v[176:179], v[80:83]\n\t"
      "v_mfma_f32_16x16x32_bf16 v[84:87], v[164:167], v[180:183], v[84:87]\n\t"
      "v_mfma_f32_16x16x32_bf16 v[88:91], v[164:167], v[184:187], v[88:91]\n\t"
      "v_mfma_f32_16x16x32_bf16 v[92:95], v[164:167], v[188:191], v[92:95]\n\t"
      STAMPASM(4)
      "s_waitcnt vmcnt(0)\n\t"
      "s_nop 7\n\t"
      : "+{v[64:95]}"(own), "=&{v[96:127]}"(oth), [k] "=&s"(sk), [so] "=&s"(sso), [t0] "=&s"(st0), [t1] "=&s"(st1), [cnt] "=&s"(scnt)
        , [q0] "=&s"(q0), [q1] "=&s"(q1), [q2] "=&s"(q2), [q3] "=&s"(q3), [q4] "=&s"(q4)
      : [wa0] "v"(wa[0]), [wa1] "v"(wa[1]), [ea0] "v"(ea[0]), [ea1] "v"(ea[1]), [l16] "v"(lane * 16), [srd] "s"(srd), [mask] "s"(mask), [e] "s"(e),
        [kd] "s"(kd), [kdw] "s"(kdw), [kp] "s"(kp), [pos] "s"(pos), [pop] "s"(pop), [mfl] "s"(mfl), [pfl] "s"(pfl), [tagA] "s"(tag1), [tagB] "s"(tag0),
        [nsm1] "i"(NS - 1)
      : "memory", "scc", "v128", "v129", "v130", "v131", "v132", "v133", "v134", "v135", "v136", "v137", "v138", "v139", "v140", "v141", "v142", "v143", "v144", "v145", "v146", "v147", "v148", "v149", "v150", "v151", "v152", "v153", "v154", "v155", "v156", "v157", "v158", "v159", "v160", "v161", "v162", "v163", "v164", "v165", "v166", "v167", "v168", "v169", "v170", "v171", "v172", "v173", "v174", "v175", "v176", "v177", "v178", "v179", "v180", "v181", "v182", "v183", "v184", "v185", "v186", "v187", "v188", "v189", "v190", "v191", "v192", "v193", "v194", "v195", "v196", "v197", "v198", "v199");
      if (w == 0 && lane == 0) {
        unsigned long long* sp = (unsigned long long*)(ws + (16 << 20)) + bid * 40;
        sp[1] = q0; sp[2] = q1; sp[3] = q2; sp[4] = q3; sp[5] = q4;
      }
    }
    if (w == 0) ST(6);
    float s[4];
#pragma unroll
    for (int m = 0; m < 4; ++m) {
      float a = 0.f;
#pragma unroll
      for (int b = 0; b < 2; ++b) {
        f32x4 v;
#pragma unroll
        for (int i = 0; i < 4; ++i) v[i] = own[(b * 4 + m) * 4 + i] + oth[(b * 4 + m) * 4 + i];
        const unsigned w0 = pw[b][0], w1 = pw[b][1];
        const s16x2 z = {0, 0};
        s16x2 h0 = __builtin_elementwise_max(__builtin_bit_cast(s16x2, __builtin_convertvector((f32x2){v[0], v[1]}, b16x2)), z);
        s16x2 h1 = __builtin_elementwise_max(__builtin_bit_cast(s16x2, __builtin_convertvector((f32x2){v[2], v[3]}, b16x2)), z);
        a = __builtin_amdgcn_fdot2_f32_bf16(__builtin_bit_cast(b16x2, h0), __builtin_bit_cast(b16x2, w0), a, false);
        a = __builtin_amdgcn_fdot2_f32_bf16(__builtin_bit_cast(b16x2, h1), __builtin_bit_cast(b16x2, w1), a, false);
      }
      s[m] = a;
    }
    float tot;
    {
      auto x = __builtin_amdgcn_permlane32_swap(__float_as_uint(s[0]), __float_as_uint(s[2]), false, false);
      auto y = __builtin_amdgcn_permlane32_swap(__float_as_uint(s[1]), __float_as_uint(s[3]), false, false);
      float t0 = __uint_as_float(x[0]) + __uint_as_float(x[1]);
      float t1 = __uint_as_float(y[0]) + __uint_as_float(y[1]);
      auto z = __builtin_amdgcn_permlane16_swap(__float_as_uint(t0), __float_as_uint(t1), false, false);
      tot = __uint_as_float(z[0]) + __uint_as_float(z[1]);
    }
    {
      u32x4 gv = {__float_as_uint(tot), tag0, __float_as_uint(tot), tag1};
      const unsigned go = (unsigned)(G * 4096 + (c8 * 2 + nq) * 256 + c16 * 16);
      __builtin_amdgcn_raw_buffer_store_b128(gv, rsS, (int)go, (int)(WS_GRAN + (unsigned)(mt * 8 + 4 * mw) * 4096u), 16);
    }
    if (w == 0) {
      ST(7);
      float acc = 0.f;
      for (int spin = 0; spin < SPIN_MAX; ++spin) {
        bool ok = true;
        acc = 0.f;
        asm volatile("" ::: "memory");
#pragma unroll
        for (int i = 0; i < 4; ++i) {
          u32x4 gv = __builtin_amdgcn_raw_buffer_load_b128(rsS, lane * 16 + i * 1024, (int)(WS_GRAN + (unsigned)gid * 4096u), 16);
          ok = ok && gv[1] == tag0 && gv[3] == tag1 && gv[0] == gv[2];
          acc += __uint_as_float(gv[0]);
        }
        if (__all(ok)) break;
      }
      auto x = __builtin_amdgcn_permlane32_swap(__float_as_uint(acc), __float_as_uint(acc), false, false);
      acc = __uint_as_float(x[0]) + __uint_as_float(x[1]);
      auto y = __builtin_amdgcn_permlane16_swap(__float_as_uint(acc), __float_as_uint(acc), false, false);
      acc = __uint_as_float(y[0]) + __uint_as_float(y[1]);
      float deep = rbf(rbf(acc) + b2v);
      float z = rbf(wide + deep);
      float o = 1.f / (1.f + __expf(-z));
      if (lane < 16) out[m0 + 16 * c8 + lane] = __builtin_bit_cast(unsigned short, __float2bfloat16(o));
      if (c8 == 0 && lane == 0) __builtin_amdgcn_raw_buffer_store_b32(e0, rsS, 0, (int)(WS_EPOCH + mt * 128), 16);
      ST(8);
    }
  }
}

__global__ void k0(char* ws) {
  unsigned long long t = __builtin_amdgcn_s_memrealtime();
  if (threadIdx.x == 0) *(unsigned long long*)(ws + (18 << 20)) = t;
}
__device__ __forceinline__ unsigned bred(unsigned v, int op, unsigned* sm) {
  for (int o = 32; o; o >>= 1) { unsigned u = __shfl_xor(v, o); v = op == 0 ? v + u : op == 1 ? (u > v ? u : v) : (u < v ? u : v); }
  __syncthreads();
  if ((threadIdx.x & 63) == 0) sm[threadIdx.x >> 6] = v;
  __syncthreads();
  unsigned a = sm[0], b = sm[1], c = sm[2], d = sm[3];
  if (op == 0) return a + b + c + d;
  if (op == 1) { a = a > b ? a : b; c = c > d ? c : d; return a > c ? a : c; }
  a = a < b ? a : b; c = c < d ? c : d; return a < c ? a : c;
}
__global__ void report(const char* ws, int metric, int amp, int last) {
  unsigned long long t0 = __builtin_amdgcn_s_memrealtime();
  __shared__ unsigned sm[4];
  const unsigned long long* st = (const unsigned long long*)(ws + (16 << 20));
  int i = threadIdx.x;
  const int k = metric % 100;
  unsigned a0 = (unsigned)(t0 - st[i * 40]), ak = (unsigned)(t0 - st[i * 40 + (k < 40 ? k : 0)]), al = (unsigned)(t0 - st[i * 40 + last]);
  unsigned tk0 = (unsigned)(t0 - *(const unsigned long long*)(ws + (18 << 20)));
  t0 = __shfl(t0, 0);
  unsigned first = bred(a0, 1, sm), lastt = bred(al, 2, sm);
  unsigned sum = bred(first - ak, 0, sm), mx = bred(first - ak, 1, sm), mn = bred(first - ak, 2, sm);
  unsigned long long m256 = metric < 100 ? sum : metric < 200 ? mx * 256ull : metric == 200 ? lastt * 256ull : metric == 201 ? (tk0 - first) * 256ull : mn * 256ull;
  if (m256 > 5000ull * 256) m256 = 5000ull * 256;
  if (i == 0) {
    unsigned long long t1 = __builtin_amdgcn_s_memrealtime();
    while (__builtin_amdgcn_s_memrealtime() - t1 < (m256 * amp) / 256) __builtin_amdgcn_s_sleep(1);
  }
}
extern "C" void kernel_launch(void* const* d_in, const int* in_sizes, int n_in, void* d_out, int out_size, void* d_ws,
                              size_t ws_size, hipStream_t stream) {
  const int* idx = (const int*)d_in[0];
  const unsigned short* table = (const unsigned short*)d_in[1];
  const unsigned short* xw = (const unsigned short*)d_in[2];
  const unsigned short* ww = (const unsigned short*)d_in[3];
  const unsigned short* bw = (const unsigned short*)d_in[4];
  const unsigned short* W1 = (const unsigned short*)d_in[5];
  const unsigned short* b1 = (const unsigned short*)d_in[6];
  const unsigned short* W2 = (const unsigned short*)d_in[7];
  const unsigned short* b2 = (const unsigned short*)d_in[8];
  int V = in_sizes[1] / D;
  (void)hipFuncSetAttribute((const void*)k1, hipFuncAttributeMaxDynamicSharedMemorySize, LDS_BYTES);
  k1<<<256, 512, LDS_BYTES, stream>>>(idx, table, xw, ww, bw, W1, b1, W2, b2, (unsigned short*)d_out, (char*)d_ws, V);
  report<<<1, 256, 0, stream>>>((const char*)d_ws, 200, 0, 8);
}
```

```cpp
#include <hip/hip_runtime.h>
#include <stdint.h>

#define MAP 1
constexpr int Bsz = 4096, F = 26, D = 128, H = 512;
constexpr int NS1 = 2 * 13, NS = 52 - NS1;
constexpr int LDS_BYTES = 10 * 16384;
constexpr unsigned WS_END = 65536 + 256u * 20480u + 32u * 8192u;

struct Row { unsigned wide; unsigned char u2, last, pad[2], u[56]; };
struct Args {
  const void* idx; unsigned idxb, f0;
  const void* W1; unsigned w1b, f1;
  const void* table; unsigned tabb, f2;
  void* ws; unsigned wsb, f3;
  const void *b1, *W2, *xw, *ww, *bw, *b2; void* out; unsigned long long pad;
  Row row[32];
};
static_assert(sizeof(Args) == 128 + 32 * 64, "no padding");

__global__ __launch_bounds__(512, 2) __attribute__((aligned(4096))) void k1(Args args) {
  asm volatile(
      "LS_%=:\n"
      "s_load_dwordx16 s[16:31], %[kp], 0x0\n"
      "v_readfirstlane_b32 s35, %[tid]\n"
      "s_lshl_b32 s34, %[bid], 3\n"
      "s_and_b32 s34, s34, 0x7c0\n"
      "s_bfe_u32 s36, %[bid], 0x20001\n"
      "s_lshr_b32 s3, %[bid], 5\n"
      "s_lshl3_add_u32 s3, s36, s3\n"
      "s_cmpk_lt_u32 s35, 0x100\n"
      "s_cbranch_scc1 LC_%=\n"
      "v_and_or_b32 v10, %[tid], 31, s34\n"
      "v_and_b32 v1, 56, %[tid]\n"
      "s_and_b32 s37, %[bid], 1\n"
      "v_and_b32 v3, 3, %[tid]\n"
      "global_load_ubyte v46, v10, %[kp] offset:136\n"
      "v_mul_u32_u24 v2, 13, v1\n"
      "s_mul_i32 s39, s37, 13\n"
      "v_mul_u32_u24 v3, 0x340, v3\n"
      "s_mul_i32 s40, s36, 3\n"
      "v_lshlrev_b32 v5, 4, %[tid]\n"
      "s_add_u32 s39, s39, s40\n"
      "v_add_u32 v3, v3, v2\n"
      "s_and_b32 s35, s35, 0xc0\n"
      "v_and_b32 v5, 0x70, v5\n"
      "s_lshl_b32 s41, s3, 8\n"
      "v_lshlrev_b32 v6, 1, %[tid]\n"
      "s_add_u32 s41, s41, s35\n"
      "v_and_b32 v6, 32, v6\n"
      "s_mul_i32 s41, s41, 52\n"
      "v_xor_b32 v6, v6, v5\n"
      "s_lshl_b32 s55, s39, 1\n"
      "s_lshr_b32 s42, %[bid], 3\n"
      "s_sub_u32 s42, s3, s42\n"
      "s_and_b32 s42, s42, 3\n"
      "s_add_u32 s55, s55, s42\n"
      "s_bfe_u32 s42, %[bid], 0x10007\n"
      "s_lshl2_add_u32 s55, s42, s55\n"
      "s_andn2_b32 s42, s55, 1\n"
      "s_lshl1_add_u32 s43, s42, s41\n"
      "s_and_b32 s54, s55, 1\n"
      "s_lshl_b32 s54, s54, 7\n"
      "v_lshl_or_b32 v7, v1, 7, v6\n"
      "s_bfe_u32 s44, %[bid], 0x20003\n"
      "v_and_b32 v6, 48, %[tid]\n"
      "s_add_u32 s44, s44, s35\n"
      "v_xor_b32 v6, v6, v5\n"
      "s_lshl1_add_u32 s44, s44, s37\n"
      "v_add_u32 v16, 0x1000, v6\n"
      "s_lshl_b32 s44, s44, 7\n"
      "v_add_u32 v18, 0x800, v6\n"
      "s_add_u32 s42, s44, 0x1000\n"
      "v_add_u32 v12, s42, v7\n"
      "s_add_u32 s42, s44, 0x2c00\n"
      "v_xad_u32 v13, v7, 64, s42\n"
      "s_xor_b32 s44, s44, 0x80\n"
      "v_xor_b32 v6, 64, v6\n"
      "s_add_u32 s42, s44, 0x800\n"
      "v_add_u32 v14, s42, v7\n"
      "s_add_u32 s42, s44, 0x2400\n"
      "v_xad_u32 v15, v7, 64, s42\n"
      "s_lshl_b32 s47, s35, 6\n"
      "v_add_u32 v17, 0xc00, v6\n"
      "s_lshl_b32 s57, s55, 16\n"
      "v_add_u32 v19, 0x400, v6\n"
      "s_mov_b32 m0, s47\n"
      "v_lshl_or_b32 v8, v1, 4, v5\n"
      "s_add_u32 s44, s47, 147456\n"
      "v_add_u32 v44, s44, v3\n"
      "s_waitcnt lgkmcnt(0)\n"
      "buffer_load_dword v9, v3, s[16:19], s43 offen\n"
      "s_add_u32 s20, s20, 0xfffff000\n"
      "s_addc_u32 s21, s21, -1\n"
      "buffer_load_dwordx4 v12, s[20:23], s57 offen lds\n"
      "buffer_load_dwordx4 v13, s[20:23], s57 offen offset:1024 lds\n"
      "buffer_load_dwordx4 v14, s[20:23], s57 offen offset:2048 lds\n"
      "buffer_load_dwordx4 v15, s[20:23], s57 offen offset:3072 lds\n"
      "s_load_dwordx4 s[4:7], %[kp], 0x50\n"
      "s_load_dwordx2 s[8:9], %[kp], 0x60\n"
      "s_add_u32 s24, s24, 0xfffff000\n"
      "s_addc_u32 s25, s25, -1\n"
      "s_lshr_b32 s38, s26, 8\n"
      "s_sub_u32 s38, s38, 16\n"
      "s_xor_b32 s58, s54, 0x80\n"
      "s_xor_b32 s56, s57, 0x10000\n"
      "s_mov_b32 m0, s44\n"
      "s_nop 0\n"
      "buffer_load_dwordx4 v8, s[16:19], s41 offen lds\n"
      "buffer_load_dwordx4 v8, s[16:19], s41 offen offset:1024 lds\n"
      "buffer_load_dwordx4 v8, s[16:19], s41 offen offset:2048 lds\n"
      "s_mov_b64 exec, 0xffff\n"
      "buffer_load_dwordx4 v8, s[16:19], s41 offen offset:3072 lds\n"
      "s_mov_b64 exec, -1\n"
      "s_lshl_b32 s39, s39, 1\n"
      "s_lshl1_add_u32 s42, s40, 26\n"
      "v_mov_b32 v47, s42\n"
      "v_mov_b32 v45, s39\n"
      "s_mov_b32 s48, 3\n"
      "s_waitcnt vmcnt(9)\n"
      "v_cmp_gt_u32 vcc, 26, v46\n"
      "s_nop 1\n"
      "v_cndmask_b32 v6, v47, v45, vcc\n"
      "v_add_u32 v46, v46, v6\n"
      "v_subrev_u32 v6, 52, v46\n"
      "v_min_u32 v46, v46, v6\n"
      "v_lshlrev_b32 v6, 1, v46\n"
      "v_lshlrev_b32 v47, 7, v46\n"
      "v_and_b32 v6, 0xfc, v6\n"
      "v_and_b32 v47, 0x80, v47\n"
      "v_lshlrev_b32 v46, 16, v46\n"
      "v_mov_b32_dpp v48, v6 wave_shl:1 row_mask:0xf bank_mask:0xf\n"
      "v_readlane_b32 s53, v47, 2\n"
      "v_readlane_b32 s42, v6, 2\n"
      "v_readlane_b32 s37, v6, 3\n"
      "v_readlane_b32 s52, v46, 2\n"
      "v_add_u32 v50, s42, v44\n"
      "v_readlane_b32 s42, v48, 3\n"
      "v_add_u32 v49, s37, v44\n"
      "v_readlane_b32 s45, v46, 3\n"
      "v_readlane_b32 s46, v47, 3\n"
      "v_add_u32 v45, s42, v44\n"
      "s_add_u32 m0, s47, 65536\n"
      "s_waitcnt vmcnt(8)\n"
      "v_ashrrev_i32 v6, 31, v9\n"
      "v_and_b32 v6, s38, v6\n"
      "v_add_lshl_u32 v9, v6, v9, 8\n"
      "s_nop 1\n"
      "v_add_u32_dpp v32, v9, v16 quad_perm:[0,0,0,0] row_mask:0xf bank_mask:0xf\n"
      "v_add_u32_dpp v33, v9, v17 quad_perm:[1,1,1,1] row_mask:0xf bank_mask:0xf\n"
      "buffer_load_dwordx4 v32, s[24:27], s54 offen lds\n"
      "v_add_u32_dpp v34, v9, v18 quad_perm:[2,2,2,2] row_mask:0xf bank_mask:0xf\n"
      "buffer_load_dwordx4 v33, s[24:27], s54 offen offset:1024 lds\n"
      "v_add_u32_dpp v35, v9, v19 quad_perm:[3,3,3,3] row_mask:0xf bank_mask:0xf\n"
      "buffer_load_dwordx4 v34, s[24:27], s54 offen offset:2048 lds\n"
      "buffer_load_dwordx4 v35, s[24:27], s54 offen offset:3072 lds\n"
      "s_add_u32 m0, s47, 16384\n"
      "s_nop 0\n"
      "buffer_load_dwordx4 v12, s[20:23], s56 offen lds\n"
      "buffer_load_dwordx4 v13, s[20:23], s56 offen offset:1024 lds\n"
      "buffer_load_dwordx4 v14, s[20:23], s56 offen offset:2048 lds\n"
      "buffer_load_dwordx4 v15, s[20:23], s56 offen offset:3072 lds\n"
      "s_add_u32 m0, s47, 81920\n"
      "s_nop 0\n"
      "buffer_load_dwordx4 v32, s[24:27], s58 offen lds\n"
      "buffer_load_dwordx4 v33, s[24:27], s58 offen offset:1024 lds\n"
      "buffer_load_dwordx4 v34, s[24:27], s58 offen offset:2048 lds\n"
      "buffer_load_dwordx4 v35, s[24:27], s58 offen offset:3072 lds\n"
      "s_waitcnt vmcnt(12)\n"
      "ds_read_b32 v50, v50\n"
      "ds_read_b32 v49, v49\n"
      "s_waitcnt lgkmcnt(0)\n"
      "v_ashrrev_i32 v52, 31, v50\n"
      "v_ashrrev_i32 v6, 31, v49\n"
      "v_and_b32 v52, s38, v52\n"
      "v_and_b32 v6, s38, v6\n"
      "v_add_lshl_u32 v50, v52, v50, 8\n"
      "v_add_lshl_u32 v49, v6, v49, 8\n"
      "s_nop 0\n"
      "v_add_u32_dpp v36, v50, v16 quad_perm:[0,0,0,0] row_mask:0xf bank_mask:0xf\n"
      "v_add_u32_dpp v37, v50, v17 quad_perm:[1,1,1,1] row_mask:0xf bank_mask:0xf\n"
      "v_add_u32_dpp v38, v50, v18 quad_perm:[2,2,2,2] row_mask:0xf bank_mask:0xf\n"
      "v_add_u32_dpp v39, v50, v19 quad_perm:[3,3,3,3] row_mask:0xf bank_mask:0xf\n"
      "v_add_u32_dpp v20, v49, v16 quad_perm:[0,0,0,0] row_mask:0xf bank_mask:0xf\n"
      "v_add_u32_dpp v21, v49, v17 quad_perm:[1,1,1,1] row_mask:0xf bank_mask:0xf\n"
      "v_add_u32_dpp v22, v49, v18 quad_perm:[2,2,2,2] row_mask:0xf bank_mask:0xf\n"
      "v_add_u32_dpp v23, v49, v19 quad_perm:[3,3,3,3] row_mask:0xf bank_mask:0xf\n"
      "s_add_u32 m0, s47, 98304\n"
      "s_nop 0\n"
      "buffer_load_dwordx4 v36, s[24:27], s53 offen lds\n"
      "buffer_load_dwordx4 v37, s[24:27], s53 offen offset:1024 lds\n"
      "buffer_load_dwordx4 v38, s[24:27], s53 offen offset:2048 lds\n"
      "buffer_load_dwordx4 v39, s[24:27], s53 offen offset:3072 lds\n"
      "s_add_u32 m0, s47, 32768\n"
      "s_nop 0\n"
      "buffer_load_dwordx4 v12, s[20:23], s52 offen lds\n"
      "buffer_load_dwordx4 v13, s[20:23], s52 offen offset:1024 lds\n"
      "buffer_load_dwordx4 v14, s[20:23], s52 offen offset:2048 lds\n"
      "buffer_load_dwordx4 v15, s[20:23], s52 offen offset:3072 lds\n"
      "s_mul_i32 s42, s35, 13\n"
      "s_mul_i32 s37, s3, 3328\n"
      "v_and_b32 v6, 63, %[tid]\n"
      "s_add_u32 s42, s42, s37\n"
      "v_lshl_add_u32 v6, v6, 4, s42\n"
      "s_add_u32 m0, s47, 131072\n"
      "s_lshr_b64 exec, -1, 12\n"
      "global_load_lds_dwordx4 v6, s[4:5]\n"
      "s_mov_b64 exec, -1\n"
      "s_load_dwordx8 s[76:83], s[6:7], 0x0\n"
      "s_load_dword s84, s[8:9], 0x0\n"
      "s_lshl_b32 s42, s3, 7\n"
      "s_load_dwordx2 s[86:87], s[28:29], s42\n"
      "s_add_u32 s47, s47, 0xc000\n"
      "s_setprio 1\n"
      "s_waitcnt vmcnt(17)\n"
      "s_branch LL_%=\n"
      "LM_%=:\n"
      "s_barrier\n"
      "s_mov_b32 m0, s47\n"
      "s_add_u32 s48, s48, 1\n"
      "buffer_load_dwordx4 v12, s[20:23], s45 offen lds\n"
      "ds_read_b32 v49, v45\n"
      "buffer_load_dwordx4 v13, s[20:23], s45 offen offset:1024 lds\n"
      "buffer_load_dwordx4 v14, s[20:23], s45 offen offset:2048 lds\n"
      "buffer_load_dwordx4 v15, s[20:23], s45 offen offset:3072 lds\n"
      "s_add_u32 m0, s47, 65536\n"
      "v_readlane_b32 s42, v48, s48\n"
      "buffer_load_dwordx4 v20, s[24:27], s46 offen lds\n"
      "s_add_u32 s47, s47, 0x4000\n"
      "s_and_b32 s47, s47, 0xffff\n"
      "buffer_load_dwordx4 v21, s[24:27], s46 offen offset:1024 lds\n"
      "v_readlane_b32 s45, v46, s48\n"
      "s_waitcnt lgkmcnt(0)\n"
      "v_ashrrev_i32 v6, 31, v49\n"
      "v_and_b32 v6, s38, v6\n"
      "v_add_u32 v45, s42, v44\n"
      "buffer_load_dwordx4 v22, s[24:27], s46 offen offset:2048 lds\n"
      "v_add_lshl_u32 v49, v6, v49, 8\n"
      "buffer_load_dwordx4 v23, s[24:27], s46 offen offset:3072 lds\n"
      "v_readlane_b32 s46, v47, s48\n"
      "v_add_u32_dpp v20, v49, v16 quad_perm:[0,0,0,0] row_mask:0xf bank_mask:0xf\n"
      "v_add_u32_dpp v21, v49, v17 quad_perm:[1,1,1,1] row_mask:0xf bank_mask:0xf\n"
      "v_add_u32_dpp v22, v49, v18 quad_perm:[2,2,2,2] row_mask:0xf bank_mask:0xf\n"
      "v_add_u32_dpp v23, v49, v19 quad_perm:[3,3,3,3] row_mask:0xf bank_mask:0xf\n"
      "s_waitcnt vmcnt(16)\n"
      "LL_%=:\n"
      "s_barrier\n"
      "LB_%=:\n"
      "s_add_u32 m0, s47, 65536\n"
      "s_add_u32 s48, s48, 1\n"
      "buffer_load_dwordx4 v20, s[24:27], s46 offen lds\n"
      "ds_read_b32 v49, v45\n"
      "buffer_load_dwordx4 v21, s[24:27], s46 offen offset:1024 lds\n"
      "v_readlane_b32 s42, v48, s48\n"
      "buffer_load_dwordx4 v22, s[24:27], s46 offen offset:2048 lds\n"
      "s_waitcnt lgkmcnt(0)\n"
      "v_ashrrev_i32 v6, 31, v49\n"
      "v_and_b32 v6, s38, v6\n"
      "buffer_load_dwordx4 v23, s[24:27], s46 offen offset:3072 lds\n"
      "s_mov_b32 m0, s47\n"
      "v_add_lshl_u32 v49, v6, v49, 8\n"
      "buffer_load_dwordx4 v12, s[20:23], s45 offen lds\n"
      "v_add_u32 v45, s42, v44\n"
      "v_readlane_b32 s46, v47, s48\n"
      "v_add_u32_dpp v20, v49, v16 quad_perm:[0,0,0,0] row_mask:0xf bank_mask:0xf\n"
      "v_add_u32_dpp v21, v49, v17 quad_perm:[1,1,1,1] row_mask:0xf bank_mask:0xf\n"
      "buffer_load_dwordx4 v13, s[20:23], s45 offen offset:1024 lds\n"
      "v_add_u32_dpp v22, v49, v18 quad_perm:[2,2,2,2] row_mask:0xf bank_mask:0xf\n"
      "v_add_u32_dpp v23, v49, v19 quad_perm:[3,3,3,3] row_mask:0xf bank_mask:0xf\n"
      "buffer_load_dwordx4 v14, s[20:23], s45 offen offset:2048 lds\n"
      "s_add_u32 s47, s47, 0x4000\n"
      "s_and_b32 s47, s47, 0xffff\n"
      "buffer_load_dwordx4 v15, s[20:23], s45 offen offset:3072 lds\n"
      "v_readlane_b32 s45, v46, s48\n"
      "s_cmp_lg_u32 s48, 22\n"
      "s_waitcnt vmcnt(16)\n"
      "s_cbranch_scc1 LM_%=\n"
      "s_lshl_b32 s49, s35, 6\n"
      "s_barrier\n"
      "s_mov_b32 m0, s47\n"
      "s_add_u32 s48, s48, 1\n"
      "buffer_load_dwordx4 v14, s[20:23], s45 offen offset:2048 lds\n"
      "ds_read_b32 v49, v45\n"
      "buffer_load_dwordx4 v15, s[20:23], s45 offen offset:3072 lds\n"
      "s_mov_b32 s14, s45\n"
      "s_add_u32 m0, s47, 65536\n"
      "v_readlane_b32 s42, v48, s48\n"
      "buffer_load_dwordx4 v20, s[24:27], s46 offen lds\n"
      "s_add_u32 s47, s47, 0x4000\n"
      "s_and_b32 s47, s47, 0xffff\n"
      "buffer_load_dwordx4 v21, s[24:27], s46 offen offset:1024 lds\n"
      "v_readlane_b32 s45, v46, s48\n"
      "s_waitcnt lgkmcnt(0)\n"
      "v_ashrrev_i32 v6, 31, v49\n"
      "v_and_b32 v6, s38, v6\n"
      "v_add_u32 v45, s42, v44\n"
      "buffer_load_dwordx4 v22, s[24:27], s46 offen offset:2048 lds\n"
      "v_add_lshl_u32 v49, v6, v49, 8\n"
      "buffer_load_dwordx4 v23, s[24:27], s46 offen offset:3072 lds\n"
      "v_readlane_b32 s46, v47, s48\n"
      "v_add_u32_dpp v20, v49, v16 quad_perm:[0,0,0,0] row_mask:0xf bank_mask:0xf\n"
      "v_add_u32_dpp v21, v49, v17 quad_perm:[1,1,1,1] row_mask:0xf bank_mask:0xf\n"
      "v_add_u32_dpp v22, v49, v18 quad_perm:[2,2,2,2] row_mask:0xf bank_mask:0xf\n"
      "v_add_u32_dpp v23, v49, v19 quad_perm:[3,3,3,3] row_mask:0xf bank_mask:0xf\n"
      "s_waitcnt vmcnt(14)\n"
      "s_barrier\n"
      "s_add_u32 m0, s47, 65536\n"
      "s_add_u32 s48, s48, 1\n"
      "buffer_load_dwordx4 v20, s[24:27], s46 offen lds\n"
      "ds_read_b32 v49, v45\n"
      "buffer_load_dwordx4 v21, s[24:27], s46 offen offset:1024 lds\n"
      "v_readlane_b32 s42, v48, s48\n"
      "buffer_load_dwordx4 v22, s[24:27], s46 offen offset:2048 lds\n"
      "s_waitcnt lgkmcnt(0)\n"
      "v_ashrrev_i32 v6, 31, v49\n"
      "v_and_b32 v6, s38, v6\n"
      "buffer_load_dwordx4 v23, s[24:27], s46 offen offset:3072 lds\n"
      "s_mov_b32 m0, s47\n"
      "v_add_lshl_u32 v49, v6, v49, 8\n"
      "buffer_load_dwordx4 v14, s[20:23], s45 offen offset:2048 lds\n"
      "v_add_u32 v45, s42, v44\n"
      "v_readlane_b32 s46, v47, s48\n"
      "v_add_u32_dpp v20, v49, v16 quad_perm:[0,0,0,0] row_mask:0xf bank_mask:0xf\n"
      "v_add_u32_dpp v21, v49, v17 quad_perm:[1,1,1,1] row_mask:0xf bank_mask:0xf\n"
      "buffer_load_dwordx4 v15, s[20:23], s45 offen offset:3072 lds\n"
      "v_add_u32_dpp v22, v49, v18 quad_perm:[2,2,2,2] row_mask:0xf bank_mask:0xf\n"
      "v_add_u32_dpp v23, v49, v19 quad_perm:[3,3,3,3] row_mask:0xf bank_mask:0xf\n"
      "s_mov_b32 s15, s45\n"
      "s_add_u32 s47, s47, 0x4000\n"
      "s_and_b32 s47, s47, 0xffff\n"
      "v_readlane_b32 s45, v46, s48\n"
      "s_xor_b32 s58, s46, 0x80\n"
      "s_xor_b32 s56, s45, 0x10000\n"
      "s_nop 0\n"
      "s_add_u32 m0, s49, 30720\n"
      "s_nop 0\n"
      "buffer_load_dwordx4 v14, s[20:23], s45 offen offset:2048 lds\n"
      "buffer_load_dwordx4 v15, s[20:23], s45 offen offset:3072 lds\n"
      "s_add_u32 m0, s49, 147456\n"
      "s_nop 0\n"
      "buffer_load_dwordx4 v20, s[24:27], s46 offen lds\n"
      "buffer_load_dwordx4 v21, s[24:27], s46 offen offset:1024 lds\n"
      "buffer_load_dwordx4 v22, s[24:27], s46 offen offset:2048 lds\n"
      "buffer_load_dwordx4 v23, s[24:27], s46 offen offset:3072 lds\n"
      "s_waitcnt vmcnt(18)\n"
      "s_barrier\n"
      "s_add_u32 m0, s49, 47104\n"
      "s_nop 0\n"
      "buffer_load_dwordx4 v14, s[20:23], s56 offen offset:2048 lds\n"
      "buffer_load_dwordx4 v15, s[20:23], s56 offen offset:3072 lds\n"
      "v_and_b32 v6, 31, %[tid]\n"
      "s_add_u32 s42, s49, 131072\n"
      "v_mad_u32_u24 v52, v6, 26, s42\n"
      "v_and_b32 v52, -4, v52\n"
      "s_mov_b32 exec_hi, 0\n"
      "ds_read2_b32 v[24:25], v52 offset0:0 offset1:1\n"
      "ds_read2_b32 v[26:27], v52 offset0:2 offset1:3\n"
      "ds_read2_b32 v[28:29], v52 offset0:4 offset1:5\n"
      "ds_read_b32 v30, v52 offset:24\n"
      "s_mov_b32 exec_hi, -1\n"
      "s_add_u32 m0, s49, 131072\n"
      "s_waitcnt lgkmcnt(0)\n"
      "buffer_load_dwordx4 v20, s[24:27], s58 offen lds\n"
      "buffer_load_dwordx4 v21, s[24:27], s58 offen offset:1024 lds\n"
      "buffer_load_dwordx4 v22, s[24:27], s58 offen offset:2048 lds\n"
      "buffer_load_dwordx4 v23, s[24:27], s58 offen offset:3072 lds\n"
      "s_add_u32 m0, s49, 2048\n"
      "s_nop 0\n"
      "buffer_load_dwordx4 v12, s[20:23], s14 offen lds\n"
      "buffer_load_dwordx4 v13, s[20:23], s14 offen offset:1024 lds\n"
      "s_waitcnt vmcnt(20)\n"
      "s_barrier\n"
      "s_add_u32 m0, s49, 18432\n"
      "s_nop 0\n"
      "buffer_load_dwordx4 v12, s[20:23], s15 offen lds\n"
      "buffer_load_dwordx4 v13, s[20:23], s15 offen offset:1024 lds\n"
      "s_waitcnt vmcnt(16)\n"
      "s_barrier\n"
      "s_add_u32 m0, s49, 34816\n"
      "s_nop 0\n"
      "buffer_load_dwordx4 v12, s[20:23], s45 offen lds\n"
      "buffer_load_dwordx4 v13, s[20:23], s45 offen offset:1024 lds\n"
      "s_add_u32 s86, s86, 1\n"
      "s_xor_b32 s86, s86, 0x5bd1e995\n"
      "s_mul_i32 s86, s86, 0x85ebca6b\n"
      "s_add_u32 s86, s86, s87\n"
      "v_mov_b32 v32, s86\n"
      "v_mov_b32 v33, 0\n"
      "s_bfe_u32 s42, %[bid], 0x20003\n"
      "s_and_b32 s37, %[bid], 1\n"
      "s_lshl1_add_u32 s42, s42, s37\n"
      "s_lshl3_add_u32 s42, s3, s42\n"
      "s_xor_b32 s37, s42, 1\n"
      "s_mul_i32 s85, s42, 20480\n"
      "s_mul_i32 s89, s37, 20480\n"
      "s_add_u32 s85, s85, 69632\n"
      "s_add_u32 s89, s89, 69632\n"
      "s_movk_i32 s44, 0x4000\n"
      "s_waitcnt vmcnt(12)\n"
      "s_barrier\n"
      "s_add_u32 m0, s49, 51200\n"
      "s_nop 0\n"
      "buffer_load_dwordx4 v12, s[20:23], s56 offen lds\n"
      "buffer_load_dwordx4 v13, s[20:23], s56 offen offset:1024 lds\n"
      "s_waitcnt vmcnt(4)\n"
      "s_barrier\n"
      "s_lshl_b32 s42, s35, 1\n"
      "v_lshl_add_u32 v6, v6, 2, s42\n"
      "s_lshl_b32 s84, s84, 16\n"
      "v_lshlrev_b32 v52, 4, %[tid]\n"
      "v_and_b32 v52, 16, v52\n"
      "s_waitcnt lgkmcnt(0)\n"
      "v_alignbit_b32 v24, v25, v24, v52\n"
      "v_alignbit_b32 v25, v26, v25, v52\n"
      "v_alignbit_b32 v26, v27, v26, v52\n"
      "v_alignbit_b32 v27, v28, v27, v52\n"
      "v_alignbit_b32 v28, v29, v28, v52\n"
      "v_alignbit_b32 v29, v30, v29, v52\n"
      "v_lshrrev_b32 v30, v52, v30\n"
      "s_lshl_b32 s42, s76, 16\n"
      "v_lshlrev_b32 v53, 16, v24\n"
      "v_mul_f32 v52, s42, v53\n"
      "s_and_b32 s42, s76, 0xffff0000\n"
      "v_and_b32 v53, 0xffff0000, v24\n"
      "v_fmac_f32 v52, s42, v53\n"
      "s_lshl_b32 s42, s77, 16\n"
      "v_lshlrev_b32 v53, 16, v25\n"
      "v_fmac_f32 v52, s42, v53\n"
      "s_and_b32 s42, s77, 0xffff0000\n"
      "v_and_b32 v53, 0xffff0000, v25\n"
      "v_fmac_f32 v52, s42, v53\n"
      "s_lshl_b32 s42, s78, 16\n"
      "v_lshlrev_b32 v53, 16, v26\n"
      "v_fmac_f32 v52, s42, v53\n"
      "s_and_b32 s42, s78, 0xffff0000\n"
      "v_and_b32 v53, 0xffff0000, v26\n"
      "v_fmac_f32 v52, s42, v53\n"
      "s_lshl_b32 s42, s79, 16\n"
      "v_lshlrev_b32 v53, 16, v27\n"
      "v_fmac_f32 v52, s42, v53\n"
      "s_cmp_lg_u32 s35, 64\n"
      "s_cbranch_scc1 LH_%=\n"
      "s_waitcnt vmcnt(0)\n"
      "s_mov_b64 exec, 1\n"
      "buffer_store_dword v32, off, s[28:31], s89 offset:256 sc1\n"
      "s_mov_b64 exec, -1\n"
      "LH_%=:\n"
      "s_and_b32 s42, s79, 0xffff0000\n"
      "v_and_b32 v53, 0xffff0000, v27\n"
      "v_fmac_f32 v52, s42, v53\n"
      "s_lshl_b32 s42, s80, 16\n"
      "v_lshlrev_b32 v53, 16, v28\n"
      "v_fmac_f32 v52, s42, v53\n"
      "s_and_b32 s42, s80, 0xffff0000\n"
      "v_and_b32 v53, 0xffff0000, v28\n"
      "v_fmac_f32 v52, s42, v53\n"
      "s_lshl_b32 s42, s81, 16\n"
      "v_lshlrev_b32 v53, 16, v29\n"
      "v_fmac_f32 v52, s42, v53\n"
      "s_and_b32 s42, s81, 0xffff0000\n"
      "v_and_b32 v53, 0xffff0000, v29\n"
      "v_fmac_f32 v52, s42, v53\n"
      "s_lshl_b32 s42, s82, 16\n"
      "v_lshlrev_b32 v53, 16, v30\n"
      "v_fmac_f32 v52, s42, v53\n"
      "v_cvt_pk_bf16_f32 v52, 0, v52\n"
      "v_add_f32 v52, s84, v52\n"
      "v_cvt_pk_bf16_f32 v52, 0, v52\n"
      "s_mov_b32 exec_hi, 0\n"
      "ds_write_b32 v6, v52 offset:32768\n"
      "s_mov_b32 exec_hi, -1\n"
      "s_waitcnt lgkmcnt(0)\n"
      "s_cmp_eq_u32 s35, 64\n"
      "s_cbranch_scc1 LG_%=\n"
      "s_waitcnt vmcnt(0)\n"
      "s_cmp_lg_u32 s35, 0\n"
      "s_cbranch_scc1 LG_%=\n"
      "s_mov_b64 exec, 1\n"
      "LP_%=:\n"
      "buffer_load_dword v33, off, s[28:31], s85 offset:256 sc1\n"
      "s_sleep 2\n"
      "s_waitcnt vmcnt(3)\n"
      "s_sub_u32 s44, s44, 1\n"
      "s_cbranch_scc1 LG_%=\n"
      "v_readfirstlane_b32 s42, v33\n"
      "s_cmp_lg_u32 s42, s86\n"
      "s_cbranch_scc1 LP_%=\n"
      "LG_%=:\n"
      "s_mov_b64 exec, -1\n"
      "s_barrier\n"
      "s_waitcnt vmcnt(0)\n"
      "s_endpgm\n"
      "LY_%=:\n"
      ".fill ((0 - (LY_%= - LS_%=) - (LU_%= - LC_%=)) & 4095) >> 2, 4, 0xbf800000\n"
      "LC_%=:\n"
      "s_load_dwordx16 s[36:51], %[kp], 0x40\n"
      "s_lshl_b32 s6, s3, 7\n"
      "s_bfe_u32 s7, %[bid], 0x20003\n"
      "s_and_b32 s4, %[bid], 1\n"
      "s_lshl1_add_u32 s4, s7, s4\n"
      "s_and_b32 s7, s35, 64\n"
      "s_lshl_b32 s5, s4, 7\n"
      "s_add_u32 s5, s5, s7\n"
      "v_and_b32 v24, 63, %[tid]\n"
      "v_lshlrev_b32 v1, 4, v24\n"
      "s_waitcnt lgkmcnt(0)\n"
      "s_load_dwordx2 s[24:25], s[28:29], s6\n"
      "s_load_dwordx16 s[52:67], s[36:37], s5\n"
      "s_load_dwordx16 s[68:83], s[38:39], s5\n"
      "s_load_dwordx8 s[16:23], s[42:43], 0x0\n"
      "s_load_dword s26, s[44:45], 0x0\n"
      "s_load_dword s27, s[46:47], 0x0\n"
      "s_lshl3_add_u32 s15, s6, 8192\n"
      "s_mov_b32 s11, 0xfffff\n"
      "s_cmp_lg_u32 s35, 0\n"
      "s_cbranch_scc1 LA_%=\n"
      "v_mov_b64 v[20:21], -1\n"
      "v_mov_b64 v[22:23], -1\n"
      "s_add_u32 s7, s15, s5\n"
      "s_mov_b64 exec, 0xff\n"
      "buffer_store_dwordx4 v[20:23], v1, s[28:31], s7 offen sc1\n"
      "s_mov_b64 exec, -1\n"
      "s_bitset0_b32 s11, 4\n"
      "LA_%=:\n"
      "v_mov_b64 v[96:97], 0\n"
      "v_mov_b64 v[98:99], 0\n"
      "v_mov_b64 v[100:101], 0\n"
      "v_mov_b64 v[102:103], 0\n"
      "v_mov_b64 v[104:105], 0\n"
      "v_mov_b64 v[106:107], 0\n"
      "v_mov_b64 v[108:109], 0\n"
      "v_mov_b64 v[110:111], 0\n"
      "v_mov_b64 v[112:113], 0\n"
      "v_mov_b64 v[114:115], 0\n"
      "v_mov_b64 v[116:117], 0\n"
      "v_mov_b64 v[118:119], 0\n"
      "v_mov_b64 v[120:121], 0\n"
      "v_mov_b64 v[122:123], 0\n"
      "v_mov_b64 v[124:125], 0\n"
      "v_mov_b64 v[126:127], 0\n"
      "s_and_b32 s7, s35, 0x80\n"
      "s_lshl2_add_u32 s15, s7, s15\n"
      "s_lshl1_add_u32 s34, s6, s7\n"
      "s_add_u32 s48, s48, s34\n"
      "s_addc_u32 s49, s49, 0\n"
      "v_lshlrev_b32 v7, 1, v24\n"
      "v_add_lshl_u32 v6, v7, s7, 1\n"
      "s_lshl3_add_u32 s7, s3, s4\n"
      "s_lshl_b32 s7, s7, 8\n"
      "s_add_u32 s7, s7, s35\n"
      "s_xor_b32 s34, s7, 0x100\n"
      "s_mul_i32 s13, s7, 80\n"
      "s_mul_i32 s14, s34, 80\n"
      "s_add_u32 s13, s13, 65536\n"
      "s_add_u32 s14, s14, 65536\n"
      "s_add_u32 s84, s6, 4096\n"
      "s_lshl3_add_u32 s85, s4, s84\n"
      "v_lshlrev_b32 v198, 3, v24\n"
      "v_lshlrev_b32 v196, 2, v24\n"
      "v_and_b32 v197, 63, v198\n"
      "v_add_u32 v196, 0x1000, v196\n"
      "v_and_b32 v2, 24, v198\n"
      "v_lshlrev_b32 v25, 2, %[tid]\n"
      "v_and_b32 v25, 0x60, v25\n"
      "v_and_b32 v26, 64, %[tid]\n"
      "v_xor_b32 v25, v25, v26\n"
      "v_lshlrev_b32 v26, 5, %[tid]\n"
      "v_and_b32 v26, 0x180, v26\n"
      "v_or3_b32 v2, v2, v25, v26\n"
      "v_lshlrev_b32 v25, 6, %[tid]\n"
      "v_and_b32 v4, 0x2000, v25\n"
      "v_and_b32 v25, 0x400, v25\n"
      "v_lshlrev_b32 v26, 7, %[tid]\n"
      "v_and_b32 v5, 0x780, v26\n"
      "v_and_b32 v26, 0x1000, v26\n"
      "v_or3_b32 v2, v2, v25, v26\n"
      "v_and_b32 v25, 48, %[tid]\n"
      "v_xor_b32 v25, v25, v198\n"
      "v_and_b32 v25, 0x70, v25\n"
      "v_or3_b32 v4, v4, v5, v25\n"
      "v_xor_b32 v3, 32, v2\n"
      "v_add_u32 v4, 65536, v4\n"
      "v_xor_b32 v5, 64, v4\n"
      "v_mov_b64 v[192:193], v[2:3]\n"
      "v_mov_b64 v[194:195], v[4:5]\n"
      "v_mov_b64 v[8:9], 0\n"
      "v_mov_b64 v[10:11], 0\n"
      "v_mov_b64 v[12:13], 0\n"
      "s_mov_b32 exec_lo, 0x00100001\n"
      "s_mov_b32 exec_hi, 0x10000100\n"
      "v_mov_b32 v10, 0x3f80\n"
      "s_lshl_b64 exec, exec, 1\n"
      "v_mov_b32 v10, 1.0\n"
      "s_lshl_b64 exec, exec, 1\n"
      "v_mov_b32 v11, 0x3f80\n"
      "s_lshl_b64 exec, exec, 1\n"
      "v_mov_b32 v11, 1.0\n"
      "s_mov_b64 exec, -1\n"
      "s_mov_b32 s86, 0\n"
      "s_mov_b32 s87, 0x41b00080\n"
      "s_mov_b32 s88, -1\n"
      "s_mov_b32 s89, 0xc1b007ff\n"
      "s_mov_b32 s90, 0x11111111\n"
      "s_mov_b32 s91, 0x11111111\n"
      "s_mov_b32 s92, 0x9e3b79b1\n"
      "s_mov_b32 s93, 0x85ebca6b\n"
      "s_mov_b32 s94, 0xc2b3ae35\n"
      "s_mov_b32 s95, 0x27d5eb2f\n"
      "s_mov_b32 s8, 0\n"
      "s_mov_b32 s9, 0\n"
      "s_mov_b32 s10, 0x10000\n"
      "s_waitcnt lgkmcnt(0)\n"
      "v_mov_b64 v[20:21], s[58:59]\n"
      "v_mov_b64 v[22:23], s[66:67]\n"
      "v_mov_b64 v[14:15], s[74:75]\n"
      "v_mov_b64 v[16:17], s[82:83]\n"
      "s_lshr_b64 exec, exec, 16\n"
      "v_mov_b64 v[20:21], s[56:57]\n"
      "v_mov_b64 v[22:23], s[64:65]\n"
      "v_mov_b64 v[14:15], s[72:73]\n"
      "v_mov_b64 v[16:17], s[80:81]\n"
      "s_lshr_b64 exec, exec, 16\n"
      "v_mov_b64 v[20:21], s[54:55]\n"
      "v_mov_b64 v[22:23], s[62:63]\n"
      "v_mov_b64 v[14:15], s[70:71]\n"
      "v_mov_b64 v[16:17], s[78:79]\n"
      "s_lshr_b64 exec, exec, 16\n"
      "v_mov_b64 v[20:21], s[52:53]\n"
      "v_mov_b64 v[22:23], s[60:61]\n"
      "v_mov_b64 v[14:15], s[68:69]\n"
      "v_mov_b64 v[16:17], s[76:77]\n"
      "s_mov_b64 exec, -1\n"
      "v_lshlrev_b32 v64, 16, v20\n"
      "v_and_b32 v65, 0xffff0000, v20\n"
      "v_lshlrev_b32 v66, 16, v21\n"
      "v_and_b32 v67, 0xffff0000, v21\n"
      "v_mov_b64 v[68:69], v[64:65]\n"
      "v_mov_b64 v[70:71], v[66:67]\n"
      "v_mov_b64 v[72:73], v[64:65]\n"
      "v_mov_b64 v[74:75], v[66:67]\n"
      "v_mov_b64 v[76:77], v[64:65]\n"
      "v_mov_b64 v[78:79], v[66:67]\n"
      "v_lshlrev_b32 v80, 16, v22\n"
      "v_and_b32 v81, 0xffff0000, v22\n"
      "v_lshlrev_b32 v82, 16, v23\n"
      "v_and_b32 v83, 0xffff0000, v23\n"
      "v_mov_b64 v[84:85], v[80:81]\n"
      "v_mov_b64 v[86:87], v[82:83]\n"
      "v_mov_b64 v[88:89], v[80:81]\n"
      "v_mov_b64 v[90:91], v[82:83]\n"
      "v_mov_b64 v[92:93], v[80:81]\n"
      "v_mov_b64 v[94:95], v[82:83]\n"
      "s_add_u32 s24, s24, 1\n"
      "s_xor_b32 s7, s24, 0x5bd1e995\n"
      "s_mul_i32 s7, s7, 0x85ebca6b\n"
      "s_add_u32 s7, s7, s25\n"
      "v_mov_b32 v18, s7\n"
      "s_mul_i32 s7, s24, 0x9e3779b1\n"
      "s_add_u32 s7, s7, 0x7f4a7c15\n"
      "v_mov_b32 v19, s7\n"
      "s_lshl_b32 s27, s27, 16\n"
      "s_mov_b32 s50, 0x46fa0000\n"
      "s_getpc_b64 s[52:53]\n"
      "LI_%=:\n"
      "v_lshlrev_b32 v30, 7, v24\n"
      "s_bfm_b64 exec, ((L8_%= - LI_%=) >> 7) + 1, 0\n"
      "global_load_dword v31, v30, s[52:53]\n"
      "s_mov_b64 exec, -1\n"
      "s_mov_b64 exec, 0\n"
      "s_branch LU_%=\n"
      "LV_%=:\n"
      "s_mov_b64 exec, -1\n"
      "s_barrier\n"
      "ds_read_b64_tr_b16 v[128:129], v192 offset:2048\n"
      "ds_read_b64_tr_b16 v[130:131], v192 offset:2560\n"
      "ds_read_b64_tr_b16 v[132:133], v192 offset:10240\n"
      "ds_read_b64_tr_b16 v[134:135], v192 offset:10752\n"
      "ds_read_b128 v[160:163], v194 offset:0\n"
      "ds_read_b128 v[164:167], v195 offset:0\n"
      "ds_read_b64_tr_b16 v[136:137], v193 offset:2048\n"
      "ds_read_b64_tr_b16 v[138:139], v193 offset:2560\n"
      "ds_read_b64_tr_b16 v[140:141], v193 offset:10240\n"
      "ds_read_b64_tr_b16 v[142:143], v193 offset:10752\n"
      "ds_read_b128 v[168:171], v194 offset:2048\n"
      "ds_read_b128 v[172:175], v195 offset:2048\n"
      "ds_read_b128 v[176:179], v194 offset:4096\n"
      "ds_read_b128 v[180:183], v195 offset:4096\n"
      "L1_%=:\n"
      "s_waitcnt lgkmcnt(4)\n"
      "v_mfma_f32_16x16x32_bf16 v[96:99], v[128:131], v[160:163], v[96:99]\n"
      "ds_read_b128 v[184:187], v194 offset:6144\n"
      "v_mfma_f32_16x16x32_bf16 v[96:99], v[132:135], v[164:167], v[96:99]\n"
      "ds_read_b128 v[188:191], v195 offset:6144\n"
      "v_mfma_f32_16x16x32_bf16 v[112:115], v[136:139], v[160:163], v[112:115]\n"
      "ds_read_b64_tr_b16 v[144:145], v192 offset:0\n"
      "v_mfma_f32_16x16x32_bf16 v[112:115], v[140:143], v[164:167], v[112:115]\n"
      "ds_read_b64_tr_b16 v[146:147], v192 offset:512\n"
      "s_waitcnt lgkmcnt(4)\n"
      "v_mfma_f32_16x16x32_bf16 v[116:119], v[136:139], v[168:171], v[116:119]\n"
      "ds_read_b64_tr_b16 v[148:149], v192 offset:8192\n"
      "s_add_u32 s9, s9, 0x4000\n"
      "v_mfma_f32_16x16x32_bf16 v[116:119], v[140:143], v[172:175], v[116:119]\n"
      "ds_read_b64_tr_b16 v[150:151], v192 offset:8704\n"
      "s_and_b32 s9, s9, 0xffff\n"
      "v_mfma_f32_16x16x32_bf16 v[100:103], v[128:131], v[168:171], v[100:103]\n"
      "ds_read_b64_tr_b16 v[152:153], v193 offset:0\n"
      "v_mfma_f32_16x16x32_bf16 v[100:103], v[132:135], v[172:175], v[100:103]\n"
      "ds_read_b64_tr_b16 v[154:155], v193 offset:512\n"
      "v_mfma_f32_16x16x32_bf16 v[104:107], v[128:131], v[176:179], v[104:107]\n"
      "ds_read_b64_tr_b16 v[156:157], v193 offset:8192\n"
      "v_mfma_f32_16x16x32_bf16 v[104:107], v[132:135], v[180:183], v[104:107]\n"
      "ds_read_b64_tr_b16 v[158:159], v193 offset:8704\n"
      "v_mfma_f32_16x16x32_bf16 v[120:123], v[136:139], v[176:179], v[120:123]\n"
      "v_mfma_f32_16x16x32_bf16 v[120:123], v[140:143], v[180:183], v[120:123]\n"
      "s_waitcnt lgkmcnt(4)\n"
      "v_mfma_f32_16x16x32_bf16 v[124:127], v[136:139], v[184:187], v[124:127]\n"
      "v_add_u32 v192, s9, v2\n"
      "v_add_u32 v193, s9, v3\n"
      "v_mfma_f32_16x16x32_bf16 v[124:127], v[140:143], v[188:191], v[124:127]\n"
      "v_add_u32 v194, s9, v4\n"
      "v_add_u32 v195, s9, v5\n"
      "v_mfma_f32_16x16x32_bf16 v[108:111], v[128:131], v[184:187], v[108:111]\n"
      "s_waitcnt lgkmcnt(0)\n"
      "v_mfma_f32_16x16x32_bf16 v[108:111], v[132:135], v[188:191], v[108:111]\n"
      "s_barrier\n"
      "v_mfma_f32_16x16x32_bf16 v[64:67], v[144:147], v[160:163], v[64:67]\n"
      "ds_read_b64_tr_b16 v[128:129], v192 offset:2048\n"
      "v_mfma_f32_16x16x32_bf16 v[64:67], v[148:151], v[164:167], v[64:67]\n"
      "ds_read_b64_tr_b16 v[130:131], v192 offset:2560\n"
      "v_mfma_f32_16x16x32_bf16 v[80:83], v[152:155], v[160:163], v[80:83]\n"
      "ds_read_b64_tr_b16 v[132:133], v192 offset:10240\n"
      "v_mfma_f32_16x16x32_bf16 v[80:83], v[156:159], v[164:167], v[80:83]\n"
      "ds_read_b64_tr_b16 v[134:135], v192 offset:10752\n"
      "v_mfma_f32_16x16x32_bf16 v[84:87], v[152:155], v[168:171], v[84:87]\n"
      "ds_read_b128 v[160:163], v194 offset:0\n"
      "v_mfma_f32_16x16x32_bf16 v[84:87], v[156:159], v[172:175], v[84:87]\n"
      "ds_read_b128 v[164:167], v195 offset:0\n"
      "v_mfma_f32_16x16x32_bf16 v[68:71], v[144:147], v[168:171], v[68:71]\n"
      "ds_read_b64_tr_b16 v[136:137], v193 offset:2048\n"
      "v_mfma_f32_16x16x32_bf16 v[68:71], v[148:151], v[172:175], v[68:71]\n"
      "ds_read_b64_tr_b16 v[138:139], v193 offset:2560\n"
      "v_mfma_f32_16x16x32_bf16 v[72:75], v[144:147], v[176:179], v[72:75]\n"
      "ds_read_b64_tr_b16 v[140:141], v193 offset:10240\n"
      "v_mfma_f32_16x16x32_bf16 v[72:75], v[148:151], v[180:183], v[72:75]\n"
      "ds_read_b64_tr_b16 v[142:143], v193 offset:10752\n"
      "v_mfma_f32_16x16x32_bf16 v[88:91], v[152:155], v[176:179], v[88:91]\n"
      "ds_read_b128 v[168:171], v194 offset:2048\n"
      "v_mfma_f32_16x16x32_bf16 v[88:91], v[156:159], v[180:183], v[88:91]\n"
      "ds_read_b128 v[172:175], v195 offset:2048\n"
      "v_mfma_f32_16x16x32_bf16 v[92:95], v[152:155], v[184:187], v[92:95]\n"
      "ds_read_b128 v[176:179], v194 offset:4096\n"
      "v_mfma_f32_16x16x32_bf16 v[92:95], v[156:159], v[188:191], v[92:95]\n"
      "ds_read_b128 v[180:183], v195 offset:4096\n"
      "v_mfma_f32_16x16x32_bf16 v[76:79], v[144:147], v[184:187], v[76:79]\n"
      "s_add_u32 s8, s8, 1\n"
      "v_mfma_f32_16x16x32_bf16 v[76:79], v[148:151], v[188:191], v[76:79]\n"
      "s_bitcmp1_b32 s11, s8\n"
      "s_cbranch_scc1 L1_%=\n"
      "s_cmp_lg_u32 s8, 4\n"
      "s_cbranch_scc1 L3_%=\n"
      "s_waitcnt vmcnt(0)\n"
      "s_mov_b64 exec, 1\n"
      "buffer_store_dwordx2 v[18:19], off, s[28:31], s85 sc1\n"
      "s_mov_b64 exec, -1\n"
      "s_branch L1_%=\n"
      "L3_%=:\n"
      "s_waitcnt lgkmcnt(4)\n"
      "v_mfma_f32_16x16x32_bf16 v[96:99], v[128:131], v[160:163], v[96:99]\n"
      "ds_read_b128 v[184:187], v4 offset:6144\n"
      "v_mfma_f32_16x16x32_bf16 v[96:99], v[132:135], v[164:167], v[96:99]\n"
      "ds_read_b128 v[188:191], v5 offset:6144\n"
      "v_mfma_f32_16x16x32_bf16 v[112:115], v[136:139], v[160:163], v[112:115]\n"
      "v_add_u32 v194, 0x10000, v4\n"
      "v_add_u32 v195, 0x10000, v5\n"
      "v_mfma_f32_16x16x32_bf16 v[112:115], v[140:143], v[164:167], v[112:115]\n"
      "s_waitcnt lgkmcnt(2)\n"
      "v_mfma_f32_16x16x32_bf16 v[116:119], v[136:139], v[168:171], v[116:119]\n"
      "v_mfma_f32_16x16x32_bf16 v[116:119], v[140:143], v[172:175], v[116:119]\n"
      "v_mfma_f32_16x16x32_bf16 v[100:103], v[128:131], v[168:171], v[100:103]\n"
      "v_mfma_f32_16x16x32_bf16 v[100:103], v[132:135], v[172:175], v[100:103]\n"
      "s_barrier\n"
      "v_mfma_f32_16x16x32_bf16 v[104:107], v[128:131], v[176:179], v[104:107]\n"
      "ds_read_b64_tr_b16 v[144:145], v2 offset:18432\n"
      "ds_read_b64_tr_b16 v[146:147], v2 offset:18944\n"
      "v_mfma_f32_16x16x32_bf16 v[104:107], v[132:135], v[180:183], v[104:107]\n"
      "ds_read_b128 v[160:163], v4 offset:16384\n"
      "ds_read_b64_tr_b16 v[148:149], v2 offset:26624\n"
      "v_mfma_f32_16x16x32_bf16 v[120:123], v[136:139], v[176:179], v[120:123]\n"
      "ds_read_b64_tr_b16 v[150:151], v2 offset:27136\n"
      "ds_read_b128 v[164:167], v5 offset:16384\n"
      "v_mfma_f32_16x16x32_bf16 v[120:123], v[140:143], v[180:183], v[120:123]\n"
      "ds_read_b64_tr_b16 v[152:153], v3 offset:18432\n"
      "ds_read_b64_tr_b16 v[154:155], v3 offset:18944\n"
      "s_waitcnt lgkmcnt(8)\n"
      "v_mfma_f32_16x16x32_bf16 v[124:127], v[136:139], v[184:187], v[124:127]\n"
      "ds_read_b64_tr_b16 v[156:157], v3 offset:26624\n"
      "ds_read_b64_tr_b16 v[158:159], v3 offset:27136\n"
      "v_mfma_f32_16x16x32_bf16 v[124:127], v[140:143], v[188:191], v[124:127]\n"
      "ds_read_b128 v[168:171], v4 offset:18432\n"
      "ds_read_b128 v[172:175], v5 offset:18432\n"
      "v_mfma_f32_16x16x32_bf16 v[108:111], v[128:131], v[184:187], v[108:111]\n"
      "ds_read_b128 v[176:179], v4 offset:20480\n"
      "v_mfma_f32_16x16x32_bf16 v[108:111], v[132:135], v[188:191], v[108:111]\n"
      "ds_read_b128 v[180:183], v5 offset:20480\n"
      "s_waitcnt lgkmcnt(10)\n"
      "v_mfma_f32_16x16x32_bf16 v[96:99], v[144:147], v[160:163], v[96:99]\n"
      "ds_read_b128 v[184:187], v4 offset:22528\n"
      "s_waitcnt lgkmcnt(9)\n"
      "v_mfma_f32_16x16x32_bf16 v[96:99], v[148:151], v[164:167], v[96:99]\n"
      "ds_read_b128 v[188:191], v5 offset:22528\n"
      "s_waitcnt lgkmcnt(8)\n"
      "v_mfma_f32_16x16x32_bf16 v[112:115], v[152:155], v[160:163], v[112:115]\n"
      "s_waitcnt lgkmcnt(6)\n"
      "v_mfma_f32_16x16x32_bf16 v[112:115], v[156:159], v[164:167], v[112:115]\n"
      "s_waitcnt lgkmcnt(4)\n"
      "v_mfma_f32_16x16x32_bf16 v[116:119], v[152:155], v[168:171], v[116:119]\n"
      "v_mfma_f32_16x16x32_bf16 v[116:119], v[156:159], v[172:175], v[116:119]\n"
      "v_mfma_f32_16x16x32_bf16 v[100:103], v[144:147], v[168:171], v[100:103]\n"
      "v_mfma_f32_16x16x32_bf16 v[100:103], v[148:151], v[172:175], v[100:103]\n"
      "s_barrier\n"
      "s_waitcnt lgkmcnt(0)\n"
      "v_mfma_f32_16x16x32_bf16 v[104:107], v[144:147], v[176:179], v[104:107]\n"
      "ds_read_b64_tr_b16 v[128:129], v2 offset:34816\n"
      "ds_read_b64_tr_b16 v[130:131], v2 offset:35328\n"
      "v_mfma_f32_16x16x32_bf16 v[104:107], v[148:151], v[180:183], v[104:107]\n"
      "ds_read_b128 v[160:163], v4 offset:32768\n"
      "ds_read_b64_tr_b16 v[132:133], v2 offset:43008\n"
      "v_mfma_f32_16x16x32_bf16 v[120:123], v[152:155], v[176:179], v[120:123]\n"
      "ds_read_b64_tr_b16 v[134:135], v2 offset:43520\n"
      "ds_read_b128 v[164:167], v5 offset:32768\n"
      "v_mfma_f32_16x16x32_bf16 v[120:123], v[156:159], v[180:183], v[120:123]\n"
      "ds_read_b64_tr_b16 v[136:137], v3 offset:34816\n"
      "ds_read_b64_tr_b16 v[138:139], v3 offset:35328\n"
      "v_mfma_f32_16x16x32_bf16 v[124:127], v[152:155], v[184:187], v[124:127]\n"
      "ds_read_b64_tr_b16 v[140:141], v3 offset:43008\n"
      "ds_read_b64_tr_b16 v[142:143], v3 offset:43520\n"
      "v_mfma_f32_16x16x32_bf16 v[124:127], v[156:159], v[188:191], v[124:127]\n"
      "ds_read_b128 v[168:171], v4 offset:34816\n"
      "ds_read_b128 v[172:175], v5 offset:34816\n"
      "v_mfma_f32_16x16x32_bf16 v[108:111], v[144:147], v[184:187], v[108:111]\n"
      "ds_read_b128 v[176:179], v4 offset:36864\n"
      "v_mfma_f32_16x16x32_bf16 v[108:111], v[148:151], v[188:191], v[108:111]\n"
      "ds_read_b128 v[180:183], v5 offset:36864\n"
      "s_waitcnt lgkmcnt(10)\n"
      "v_mfma_f32_16x16x32_bf16 v[96:99], v[128:131], v[160:163], v[96:99]\n"
      "ds_read_b128 v[184:187], v4 offset:38912\n"
      "s_waitcnt lgkmcnt(9)\n"
      "v_mfma_f32_16x16x32_bf16 v[96:99], v[132:135], v[164:167], v[96:99]\n"
      "ds_read_b128 v[188:191], v5 offset:38912\n"
      "s_waitcnt lgkmcnt(8)\n"
      "v_mfma_f32_16x16x32_bf16 v[112:115], v[136:139], v[160:163], v[112:115]\n"
      "s_waitcnt lgkmcnt(6)\n"
      "v_mfma_f32_16x16x32_bf16 v[112:115], v[140:143], v[164:167], v[112:115]\n"
      "s_waitcnt lgkmcnt(4)\n"
      "v_mfma_f32_16x16x32_bf16 v[116:119], v[136:139], v[168:171], v[116:119]\n"
      "v_mfma_f32_16x16x32_bf16 v[116:119], v[140:143], v[172:175], v[116:119]\n"
      "v_mfma_f32_16x16x32_bf16 v[100:103], v[128:131], v[168:171], v[100:103]\n"
      "v_mfma_f32_16x16x32_bf16 v[100:103], v[132:135], v[172:175], v[100:103]\n"
      "s_barrier\n"
      "s_waitcnt lgkmcnt(0)\n"
      "v_mfma_f32_16x16x32_bf16 v[104:107], v[128:131], v[176:179], v[104:107]\n"
      "ds_read_b64_tr_b16 v[144:145], v2 offset:51200\n"
      "ds_read_b64_tr_b16 v[146:147], v2 offset:51712\n"
      "v_mfma_f32_16x16x32_bf16 v[104:107], v[132:135], v[180:183], v[104:107]\n"
      "ds_read_b128 v[160:163], v4 offset:49152\n"
      "ds_read_b64_tr_b16 v[148:149], v2 offset:59392\n"
      "v_mfma_f32_16x16x32_bf16 v[120:123], v[136:139], v[176:179], v[120:123]\n"
      "ds_read_b64_tr_b16 v[150:151], v2 offset:59904\n"
      "ds_read_b128 v[164:167], v5 offset:49152\n"
      "v_mfma_f32_16x16x32_bf16 v[120:123], v[140:143], v[180:183], v[120:123]\n"
      "ds_read_b64_tr_b16 v[152:153], v3 offset:51200\n"
      "ds_read_b64_tr_b16 v[154:155], v3 offset:51712\n"
      "v_mfma_f32_16x16x32_bf16 v[124:127], v[136:139], v[184:187], v[124:127]\n"
      "ds_read_b64_tr_b16 v[156:157], v3 offset:59392\n"
      "ds_read_b64_tr_b16 v[158:159], v3 offset:59904\n"
      "v_mfma_f32_16x16x32_bf16 v[124:127], v[140:143], v[188:191], v[124:127]\n"
      "ds_read_b128 v[168:171], v4 offset:51200\n"
      "ds_read_b128 v[172:175], v5 offset:51200\n"
      "v_mfma_f32_16x16x32_bf16 v[108:111], v[128:131], v[184:187], v[108:111]\n"
      "ds_read_b128 v[176:179], v4 offset:53248\n"
      "v_mfma_f32_16x16x32_bf16 v[108:111], v[132:135], v[188:191], v[108:111]\n"
      "ds_read_b128 v[180:183], v5 offset:53248\n"
      "s_waitcnt lgkmcnt(10)\n"
      "v_mfma_f32_16x16x32_bf16 v[96:99], v[144:147], v[160:163], v[96:99]\n"
      "ds_read_b128 v[184:187], v4 offset:55296\n"
      "s_waitcnt lgkmcnt(9)\n"
      "v_mfma_f32_16x16x32_bf16 v[96:99], v[148:151], v[164:167], v[96:99]\n"
      "ds_read_b128 v[188:191], v5 offset:55296\n"
      "s_waitcnt lgkmcnt(8)\n"
      "v_mfma_f32_16x16x32_bf16 v[112:115], v[152:155], v[160:163], v[112:115]\n"
      "s_waitcnt lgkmcnt(6)\n"
      "v_mfma_f32_16x16x32_bf16 v[112:115], v[156:159], v[164:167], v[112:115]\n"
      "s_waitcnt lgkmcnt(4)\n"
      "v_mfma_f32_16x16x32_bf16 v[116:119], v[152:155], v[168:171], v[116:119]\n"
      "v_mfma_f32_16x16x32_bf16 v[116:119], v[156:159], v[172:175], v[116:119]\n"
      "v_mfma_f32_16x16x32_bf16 v[100:103], v[144:147], v[168:171], v[100:103]\n"
      "v_mfma_f32_16x16x32_bf16 v[100:103], v[148:151], v[172:175], v[100:103]\n"
      "s_barrier\n"
      "s_waitcnt lgkmcnt(0)\n"
      "v_mfma_f32_16x16x32_bf16 v[104:107], v[144:147], v[176:179], v[104:107]\n"
      "ds_read_b64_tr_b16 v[128:129], v2 offset:32768\n"
      "ds_read_b64_tr_b16 v[130:131], v2 offset:33280\n"
      "v_mfma_f32_16x16x32_bf16 v[104:107], v[148:151], v[180:183], v[104:107]\n"
      "ds_read_b128 v[160:163], v194 offset:16384\n"
      "ds_read_b64_tr_b16 v[132:133], v2 offset:40960\n"
      "v_mfma_f32_16x16x32_bf16 v[120:123], v[152:155], v[176:179], v[120:123]\n"
      "ds_read_b64_tr_b16 v[134:135], v2 offset:41472\n"
      "ds_read_b128 v[164:167], v195 offset:16384\n"
      "v_mfma_f32_16x16x32_bf16 v[120:123], v[156:159], v[180:183], v[120:123]\n"
      "ds_read_b64_tr_b16 v[136:137], v3 offset:32768\n"
      "ds_read_b64_tr_b16 v[138:139], v3 offset:33280\n"
      "v_mfma_f32_16x16x32_bf16 v[124:127], v[152:155], v[184:187], v[124:127]\n"
      "ds_read_b64_tr_b16 v[140:141], v3 offset:40960\n"
      "ds_read_b64_tr_b16 v[142:143], v3 offset:41472\n"
      "v_mfma_f32_16x16x32_bf16 v[124:127], v[156:159], v[188:191], v[124:127]\n"
      "ds_read_b128 v[168:171], v194 offset:18432\n"
      "ds_read_b128 v[172:175], v195 offset:18432\n"
      "v_mfma_f32_16x16x32_bf16 v[108:111], v[144:147], v[184:187], v[108:111]\n"
      "ds_read_b128 v[176:179], v194 offset:20480\n"
      "v_mfma_f32_16x16x32_bf16 v[108:111], v[148:151], v[188:191], v[108:111]\n"
      "ds_read_b128 v[180:183], v195 offset:20480\n"
      "s_waitcnt lgkmcnt(10)\n"
      "v_mfma_f32_16x16x32_bf16 v[96:99], v[128:131], v[160:163], v[96:99]\n"
      "ds_read_b128 v[184:187], v194 offset:22528\n"
      "s_waitcnt lgkmcnt(9)\n"
      "v_mfma_f32_16x16x32_bf16 v[96:99], v[132:135], v[164:167], v[96:99]\n"
      "ds_read_b128 v[188:191], v195 offset:22528\n"
      "s_waitcnt lgkmcnt(8)\n"
      "v_mfma_f32_16x16x32_bf16 v[112:115], v[136:139], v[160:163], v[112:115]\n"
      "s_waitcnt lgkmcnt(6)\n"
      "v_mfma_f32_16x16x32_bf16 v[112:115], v[140:143], v[164:167], v[112:115]\n"
      "s_waitcnt lgkmcnt(4)\n"
      "v_mfma_f32_16x16x32_bf16 v[116:119], v[136:139], v[168:171], v[116:119]\n"
      "v_mfma_f32_16x16x32_bf16 v[116:119], v[140:143], v[172:175], v[116:119]\n"
      "v_mfma_f32_16x16x32_bf16 v[100:103], v[128:131], v[168:171], v[100:103]\n"
      "v_mfma_f32_16x16x32_bf16 v[100:103], v[132:135], v[172:175], v[100:103]\n"
      "s_barrier\n"
      "s_waitcnt lgkmcnt(0)\n"
      "v_mfma_f32_16x16x32_bf16 v[104:107], v[128:131], v[176:179], v[104:107]\n"
      "ds_read_b64_tr_b16 v[144:145], v2 offset:49152\n"
      "ds_read_b64_tr_b16 v[146:147], v2 offset:49664\n"
      "v_mfma_f32_16x16x32_bf16 v[104:107], v[132:135], v[180:183], v[104:107]\n"
      "ds_read_b128 v[160:163], v194 offset:0\n"
      "ds_read_b64_tr_b16 v[148:149], v2 offset:57344\n"
      "v_mfma_f32_16x16x32_bf16 v[120:123], v[136:139], v[176:179], v[120:123]\n"
      "ds_read_b64_tr_b16 v[150:151], v2 offset:57856\n"
      "ds_read_b128 v[164:167], v195 offset:0\n"
      "v_mfma_f32_16x16x32_bf16 v[120:123], v[140:143], v[180:183], v[120:123]\n"
      "ds_read_b64_tr_b16 v[152:153], v3 offset:49152\n"
      "ds_read_b64_tr_b16 v[154:155], v3 offset:49664\n"
      "v_mfma_f32_16x16x32_bf16 v[124:127], v[136:139], v[184:187], v[124:127]\n"
      "ds_read_b64_tr_b16 v[156:157], v3 offset:57344\n"
      "ds_read_b64_tr_b16 v[158:159], v3 offset:57856\n"
      "v_mfma_f32_16x16x32_bf16 v[124:127], v[140:143], v[188:191], v[124:127]\n"
      "ds_read_b128 v[168:171], v194 offset:2048\n"
      "ds_read_b128 v[172:175], v195 offset:2048\n"
      "v_mfma_f32_16x16x32_bf16 v[108:111], v[128:131], v[184:187], v[108:111]\n"
      "ds_read_b128 v[176:179], v194 offset:4096\n"
      "v_mfma_f32_16x16x32_bf16 v[108:111], v[132:135], v[188:191], v[108:111]\n"
      "ds_read_b128 v[180:183], v195 offset:4096\n"
      "s_waitcnt lgkmcnt(10)\n"
      "v_mfma_f32_16x16x32_bf16 v[96:99], v[144:147], v[160:163], v[96:99]\n"
      "ds_read_b128 v[184:187], v194 offset:6144\n"
      "s_waitcnt lgkmcnt(9)\n"
      "v_mfma_f32_16x16x32_bf16 v[96:99], v[148:151], v[164:167], v[96:99]\n"
      "ds_read_b128 v[188:191], v195 offset:6144\n"
      "s_waitcnt lgkmcnt(8)\n"
      "v_mfma_f32_16x16x32_bf16 v[112:115], v[152:155], v[160:163], v[112:115]\n"
      "ds_read_b64_tr_b16 v[128:129], v2 offset:0\n"
      "s_waitcnt lgkmcnt(7)\n"
      "v_mfma_f32_16x16x32_bf16 v[112:115], v[156:159], v[164:167], v[112:115]\n"
      "ds_read_b64_tr_b16 v[130:131], v2 offset:512\n"
      "s_waitcnt lgkmcnt(6)\n"
      "v_mfma_f32_16x16x32_bf16 v[116:119], v[152:155], v[168:171], v[116:119]\n"
      "ds_read_b128 v[160:163], v4 offset:0\n"
      "v_mfma_f32_16x16x32_bf16 v[116:119], v[156:159], v[172:175], v[116:119]\n"
      "ds_read_b64_tr_b16 v[132:133], v2 offset:8192\n"
      "v_cvt_pk_bf16_f32 v200, v96, v97\n"
      "v_cvt_pk_bf16_f32 v201, v98, v99\n"
      "v_mfma_f32_16x16x32_bf16 v[100:103], v[144:147], v[168:171], v[100:103]\n"
      "ds_read_b64_tr_b16 v[134:135], v2 offset:8704\n"
      "v_cvt_pk_bf16_f32 v208, v112, v113\n"
      "v_cvt_pk_bf16_f32 v209, v114, v115\n"
      "v_xad_u32 v199, v200, s92, v19\n"
      "v_mfma_f32_16x16x32_bf16 v[100:103], v[148:151], v[172:175], v[100:103]\n"
      "ds_read_b128 v[164:167], v5 offset:0\n"
      "v_xad_u32 v199, v201, s93, v199\n"
      "v_xad_u32 v199, v208, s92, v199\n"
      "s_waitcnt lgkmcnt(6)\n"
      "v_mfma_f32_16x16x32_bf16 v[104:107], v[144:147], v[176:179], v[104:107]\n"
      "ds_read_b64_tr_b16 v[136:137], v3 offset:0\n"
      "v_cvt_pk_bf16_f32 v210, v116, v117\n"
      "v_cvt_pk_bf16_f32 v211, v118, v119\n"
      "v_mfma_f32_16x16x32_bf16 v[104:107], v[148:151], v[180:183], v[104:107]\n"
      "ds_read_b64_tr_b16 v[138:139], v3 offset:512\n"
      "v_xad_u32 v199, v209, s93, v199\n"
      "v_xad_u32 v199, v210, s94, v199\n"
      "v_mfma_f32_16x16x32_bf16 v[120:123], v[152:155], v[176:179], v[120:123]\n"
      "ds_read_b64_tr_b16 v[140:141], v3 offset:8192\n"
      "v_cvt_pk_bf16_f32 v202, v100, v101\n"
      "v_cvt_pk_bf16_f32 v203, v102, v103\n"
      "v_mfma_f32_16x16x32_bf16 v[120:123], v[156:159], v[180:183], v[120:123]\n"
      "ds_read_b64_tr_b16 v[142:143], v3 offset:8704\n"
      "buffer_store_dwordx4 v[208:211], v1, s[28:31], s14 offen offset:2048 sc1\n"
      "v_xad_u32 v199, v211, s95, v199\n"
      "v_mfma_f32_16x16x32_bf16 v[124:127], v[152:155], v[184:187], v[124:127]\n"
      "ds_read_b128 v[168:171], v4 offset:2048\n"
      "v_cvt_pk_bf16_f32 v204, v104, v105\n"
      "v_cvt_pk_bf16_f32 v205, v106, v107\n"
      "v_mfma_f32_16x16x32_bf16 v[124:127], v[156:159], v[188:191], v[124:127]\n"
      "ds_read_b128 v[172:175], v5 offset:2048\n"
      "buffer_store_dwordx4 v[200:203], v1, s[28:31], s14 offen offset:0 sc1\n"
      "v_xad_u32 v199, v202, s94, v199\n"
      "v_mfma_f32_16x16x32_bf16 v[108:111], v[144:147], v[184:187], v[108:111]\n"
      "ds_read_b128 v[176:179], v4 offset:4096\n"
      "v_cvt_pk_bf16_f32 v212, v120, v121\n"
      "v_cvt_pk_bf16_f32 v213, v122, v123\n"
      "v_mfma_f32_16x16x32_bf16 v[108:111], v[148:151], v[188:191], v[108:111]\n"
      "ds_read_b128 v[180:183], v5 offset:4096\n"
      "v_xad_u32 v199, v203, s95, v199\n"
      "v_xad_u32 v199, v204, s92, v199\n"
      "s_waitcnt lgkmcnt(6)\n"
      "v_mfma_f32_16x16x32_bf16 v[64:67], v[128:131], v[160:163], v[64:67]\n"
      "ds_read_b128 v[184:187], v4 offset:6144\n"
      "v_cvt_pk_bf16_f32 v214, v124, v125\n"
      "v_cvt_pk_bf16_f32 v215, v126, v127\n"
      "v_mfma_f32_16x16x32_bf16 v[64:67], v[132:135], v[164:167], v[64:67]\n"
      "ds_read_b128 v[188:191], v5 offset:6144\n"
      "v_xad_u32 v199, v205, s93, v199\n"
      "v_xad_u32 v199, v212, s92, v199\n"
      "v_mfma_f32_16x16x32_bf16 v[80:83], v[136:139], v[160:163], v[80:83]\n"
      "v_cvt_pk_bf16_f32 v206, v108, v109\n"
      "v_cvt_pk_bf16_f32 v207, v110, v111\n"
      "buffer_store_dwordx4 v[212:215], v1, s[28:31], s14 offen offset:3072 sc1\n"
      "s_waitcnt lgkmcnt(5)\n"
      "v_mfma_f32_16x16x32_bf16 v[80:83], v[140:143], v[164:167], v[80:83]\n"
      "buffer_store_dwordx4 v[204:207], v1, s[28:31], s14 offen offset:1024 sc1\n"
      "v_xad_u32 v199, v213, s93, v199\n"
      "v_xad_u32 v199, v214, s94, v199\n"
      "v_mfma_f32_16x16x32_bf16 v[84:87], v[136:139], v[168:171], v[84:87]\n"
      "v_xad_u32 v199, v215, s95, v199\n"
      "v_xad_u32 v199, v206, s94, v199\n"
      "v_xad_u32 v199, v207, s95, v199\n"
      "s_waitcnt lgkmcnt(3)\n"
      "v_mfma_f32_16x16x32_bf16 v[84:87], v[140:143], v[172:175], v[84:87]\n"
      "buffer_store_dword v199, v196, s[28:31], s14 offen sc1\n"
      "v_mfma_f32_16x16x32_bf16 v[68:71], v[128:131], v[168:171], v[68:71]\n"
      "v_mfma_f32_16x16x32_bf16 v[68:71], v[132:135], v[172:175], v[68:71]\n"
      "v_mfma_f32_16x16x32_bf16 v[72:75], v[128:131], v[176:179], v[72:75]\n"
      "ds_read_b64_tr_b16 v[144:145], v2 offset:16384\n"
      "ds_read_b64_tr_b16 v[146:147], v2 offset:16896\n"
      "s_waitcnt lgkmcnt(2)\n"
      "v_mfma_f32_16x16x32_bf16 v[72:75], v[132:135], v[180:183], v[72:75]\n"
      "ds_read_b128 v[160:163], v4 offset:16384\n"
      "ds_read_b64_tr_b16 v[148:149], v2 offset:24576\n"
      "v_mfma_f32_16x16x32_bf16 v[88:91], v[136:139], v[176:179], v[88:91]\n"
      "ds_read_b64_tr_b16 v[150:151], v2 offset:25088\n"
      "ds_read_b128 v[164:167], v5 offset:16384\n"
      "v_mfma_f32_16x16x32_bf16 v[88:91], v[140:143], v[180:183], v[88:91]\n"
      "ds_read_b64_tr_b16 v[152:153], v3 offset:16384\n"
      "ds_read_b64_tr_b16 v[154:155], v3 offset:16896\n"
      "v_mfma_f32_16x16x32_bf16 v[92:95], v[136:139], v[184:187], v[92:95]\n"
      "ds_read_b64_tr_b16 v[156:157], v3 offset:24576\n"
      "ds_read_b64_tr_b16 v[158:159], v3 offset:25088\n"
      "v_mfma_f32_16x16x32_bf16 v[92:95], v[140:143], v[188:191], v[92:95]\n"
      "ds_read_b128 v[168:171], v4 offset:18432\n"
      "ds_read_b128 v[172:175], v5 offset:18432\n"
      "v_mfma_f32_16x16x32_bf16 v[76:79], v[128:131], v[184:187], v[76:79]\n"
      "ds_read_b128 v[176:179], v4 offset:20480\n"
      "v_mfma_f32_16x16x32_bf16 v[76:79], v[132:135], v[188:191], v[76:79]\n"
      "ds_read_b128 v[180:183], v5 offset:20480\n"
      "s_waitcnt lgkmcnt(10)\n"
      "v_mfma_f32_16x16x32_bf16 v[64:67], v[144:147], v[160:163], v[64:67]\n"
      "ds_read_b128 v[184:187], v4 offset:22528\n"
      "s_waitcnt lgkmcnt(9)\n"
      "v_mfma_f32_16x16x32_bf16 v[64:67], v[148:151], v[164:167], v[64:67]\n"
      "ds_read_b128 v[188:191], v5 offset:22528\n"
      "s_waitcnt lgkmcnt(8)\n"
      "v_mfma_f32_16x16x32_bf16 v[80:83], v[152:155], v[160:163], v[80:83]\n"
      "ds_read_b64_tr_b16 v[128:129], v2 offset:2048\n"
      "s_waitcnt lgkmcnt(7)\n"
      "v_mfma_f32_16x16x32_bf16 v[80:83], v[156:159], v[164:167], v[80:83]\n"
      "ds_read_b64_tr_b16 v[130:131], v2 offset:2560\n"
      "s_waitcnt lgkmcnt(6)\n"
      "v_mfma_f32_16x16x32_bf16 v[84:87], v[152:155], v[168:171], v[84:87]\n"
      "ds_read_b128 v[160:163], v4 offset:32768\n"
      "v_mfma_f32_16x16x32_bf16 v[84:87], v[156:159], v[172:175], v[84:87]\n"
      "ds_read_b64_tr_b16 v[132:133], v2 offset:10240\n"
      "v_mfma_f32_16x16x32_bf16 v[68:71], v[144:147], v[168:171], v[68:71]\n"
      "ds_read_b64_tr_b16 v[134:135], v2 offset:10752\n"
      "v_mfma_f32_16x16x32_bf16 v[68:71], v[148:151], v[172:175], v[68:71]\n"
      "ds_read_b128 v[164:167], v5 offset:32768\n"
      "s_waitcnt lgkmcnt(6)\n"
      "v_mfma_f32_16x16x32_bf16 v[72:75], v[144:147], v[176:179], v[72:75]\n"
      "ds_read_b64_tr_b16 v[136:137], v3 offset:2048\n"
      "v_mfma_f32_16x16x32_bf16 v[72:75], v[148:151], v[180:183], v[72:75]\n"
      "ds_read_b64_tr_b16 v[138:139], v3 offset:2560\n"
      "v_mfma_f32_16x16x32_bf16 v[88:91], v[152:155], v[176:179], v[88:91]\n"
      "ds_read_b64_tr_b16 v[140:141], v3 offset:10240\n"
      "v_mfma_f32_16x16x32_bf16 v[88:91], v[156:159], v[180:183], v[88:91]\n"
      "ds_read_b64_tr_b16 v[142:143], v3 offset:10752\n"
      "v_mfma_f32_16x16x32_bf16 v[92:95], v[152:155], v[184:187], v[92:95]\n"
      "ds_read_b128 v[168:171], v4 offset:34816\n"
      "v_mfma_f32_16x16x32_bf16 v[92:95], v[156:159], v[188:191], v[92:95]\n"
      "ds_read_b128 v[172:175], v5 offset:34816\n"
      "v_mfma_f32_16x16x32_bf16 v[76:79], v[144:147], v[184:187], v[76:79]\n"
      "ds_read_b128 v[176:179], v4 offset:36864\n"
      "v_mfma_f32_16x16x32_bf16 v[76:79], v[148:151], v[188:191], v[76:79]\n"
      "ds_read_b128 v[180:183], v5 offset:36864\n"
      "s_waitcnt lgkmcnt(6)\n"
      "v_mfma_f32_16x16x32_bf16 v[64:67], v[128:131], v[160:163], v[64:67]\n"
      "ds_read_b128 v[184:187], v4 offset:38912\n"
      "v_mfma_f32_16x16x32_bf16 v[64:67], v[132:135], v[164:167], v[64:67]\n"
      "ds_read_b128 v[188:191], v5 offset:38912\n"
      "v_mfma_f32_16x16x32_bf16 v[80:83], v[136:139], v[160:163], v[80:83]\n"
      "s_waitcnt lgkmcnt(5)\n"
      "v_mfma_f32_16x16x32_bf16 v[80:83], v[140:143], v[164:167], v[80:83]\n"
      "v_mfma_f32_16x16x32_bf16 v[84:87], v[136:139], v[168:171], v[84:87]\n"
      "s_waitcnt lgkmcnt(3)\n"
      "v_mfma_f32_16x16x32_bf16 v[84:87], v[140:143], v[172:175], v[84:87]\n"
      "v_mfma_f32_16x16x32_bf16 v[68:71], v[128:131], v[168:171], v[68:71]\n"
      "v_mfma_f32_16x16x32_bf16 v[68:71], v[132:135], v[172:175], v[68:71]\n"
      "s_barrier\n"
      "buffer_load_dwordx4 v[96:99], v1, s[28:31], s13 offen offset:0 sc1\n"
      "buffer_load_dwordx4 v[100:103], v1, s[28:31], s13 offen offset:1024 sc1\n"
      "buffer_load_dwordx4 v[104:107], v1, s[28:31], s13 offen offset:2048 sc1\n"
      "buffer_load_dwordx4 v[108:111], v1, s[28:31], s13 offen offset:3072 sc1\n"
      "buffer_load_dword v112, v196, s[28:31], s13 offen sc1\n"
      "buffer_load_dwordx2 v[114:115], v197, s[28:31], s84 offen sc1\n"
      "v_mfma_f32_16x16x32_bf16 v[72:75], v[128:131], v[176:179], v[72:75]\n"
      "ds_read_b64_tr_b16 v[144:145], v2 offset:18432\n"
      "ds_read_b64_tr_b16 v[146:147], v2 offset:18944\n"
      "s_waitcnt lgkmcnt(2)\n"
      "v_mfma_f32_16x16x32_bf16 v[72:75], v[132:135], v[180:183], v[72:75]\n"
      "ds_read_b128 v[160:163], v4 offset:49152\n"
      "ds_read_b64_tr_b16 v[148:149], v2 offset:26624\n"
      "v_mfma_f32_16x16x32_bf16 v[88:91], v[136:139], v[176:179], v[88:91]\n"
      "ds_read_b64_tr_b16 v[150:151], v2 offset:27136\n"
      "ds_read_b128 v[164:167], v5 offset:49152\n"
      "v_mfma_f32_16x16x32_bf16 v[88:91], v[140:143], v[180:183], v[88:91]\n"
      "ds_read_b64_tr_b16 v[152:153], v3 offset:18432\n"
      "ds_read_b64_tr_b16 v[154:155], v3 offset:18944\n"
      "v_mfma_f32_16x16x32_bf16 v[92:95], v[136:139], v[184:187], v[92:95]\n"
      "ds_read_b64_tr_b16 v[156:157], v3 offset:26624\n"
      "ds_read_b64_tr_b16 v[158:159], v3 offset:27136\n"
      "v_mfma_f32_16x16x32_bf16 v[92:95], v[140:143], v[188:191], v[92:95]\n"
      "ds_read_b128 v[168:171], v4 offset:51200\n"
      "ds_read_b128 v[172:175], v5 offset:51200\n"
      "v_mfma_f32_16x16x32_bf16 v[76:79], v[128:131], v[184:187], v[76:79]\n"
      "ds_read_b128 v[176:179], v4 offset:53248\n"
      "v_mfma_f32_16x16x32_bf16 v[76:79], v[132:135], v[188:191], v[76:79]\n"
      "ds_read_b128 v[180:183], v5 offset:53248\n"
      "s_waitcnt lgkmcnt(10)\n"
      "v_mfma_f32_16x16x32_bf16 v[64:67], v[144:147], v[160:163], v[64:67]\n"
      "ds_read_b128 v[184:187], v4 offset:55296\n"
      "s_waitcnt lgkmcnt(9)\n"
      "v_mfma_f32_16x16x32_bf16 v[64:67], v[148:151], v[164:167], v[64:67]\n"
      "ds_read_b128 v[188:191], v5 offset:55296\n"
      "s_waitcnt lgkmcnt(8)\n"
      "v_mfma_f32_16x16x32_bf16 v[80:83], v[152:155], v[160:163], v[80:83]\n"
      "ds_read_b64_tr_b16 v[128:129], v2 offset:34816\n"
      "s_waitcnt lgkmcnt(7)\n"
      "v_mfma_f32_16x16x32_bf16 v[80:83], v[156:159], v[164:167], v[80:83]\n"
      "ds_read_b64_tr_b16 v[130:131], v2 offset:35328\n"
      "s_waitcnt lgkmcnt(6)\n"
      "v_mfma_f32_16x16x32_bf16 v[84:87], v[152:155], v[168:171], v[84:87]\n"
      "ds_read_b128 v[160:163], v194 offset:16384\n"
      "v_mfma_f32_16x16x32_bf16 v[84:87], v[156:159], v[172:175], v[84:87]\n"
      "ds_read_b64_tr_b16 v[132:133], v2 offset:43008\n"
      "v_mfma_f32_16x16x32_bf16 v[68:71], v[144:147], v[168:171], v[68:71]\n"
      "ds_read_b64_tr_b16 v[134:135], v2 offset:43520\n"
      "v_mfma_f32_16x16x32_bf16 v[68:71], v[148:151], v[172:175], v[68:71]\n"
      "ds_read_b128 v[164:167], v195 offset:16384\n"
      "s_waitcnt lgkmcnt(6)\n"
      "v_mfma_f32_16x16x32_bf16 v[72:75], v[144:147], v[176:179], v[72:75]\n"
      "ds_read_b64_tr_b16 v[136:137], v3 offset:34816\n"
      "v_mfma_f32_16x16x32_bf16 v[72:75], v[148:151], v[180:183], v[72:75]\n"
      "ds_read_b64_tr_b16 v[138:139], v3 offset:35328\n"
      "v_mfma_f32_16x16x32_bf16 v[88:91], v[152:155], v[176:179], v[88:91]\n"
      "ds_read_b64_tr_b16 v[140:141], v3 offset:43008\n"
      "v_mfma_f32_16x16x32_bf16 v[88:91], v[156:159], v[180:183], v[88:91]\n"
      "ds_read_b64_tr_b16 v[142:143], v3 offset:43520\n"
      "v_mfma_f32_16x16x32_bf16 v[92:95], v[152:155], v[184:187], v[92:95]\n"
      "ds_read_b128 v[168:171], v194 offset:18432\n"
      "v_mfma_f32_16x16x32_bf16 v[92:95], v[156:159], v[188:191], v[92:95]\n"
      "ds_read_b128 v[172:175], v195 offset:18432\n"
      "v_mfma_f32_16x16x32_bf16 v[76:79], v[144:147], v[184:187], v[76:79]\n"
      "ds_read_b128 v[176:179], v194 offset:20480\n"
      "v_mfma_f32_16x16x32_bf16 v[76:79], v[148:151], v[188:191], v[76:79]\n"
      "ds_read_b128 v[180:183], v195 offset:20480\n"
      "LU_%=:\n"
      "s_cbranch_execz LV_%=\n"
      "s_waitcnt lgkmcnt(6)\n"
      "v_mfma_f32_16x16x32_bf16 v[64:67], v[128:131], v[160:163], v[64:67]\n"
      "ds_read_b128 v[184:187], v194 offset:22528\n"
      "v_mfma_f32_16x16x32_bf16 v[64:67], v[132:135], v[164:167], v[64:67]\n"
      "ds_read_b128 v[188:191], v195 offset:22528\n"
      "v_mfma_f32_16x16x32_bf16 v[80:83], v[136:139], v[160:163], v[80:83]\n"
      "ds_read_b64_tr_b16 v[144:145], v2 offset:51200\n"
      "s_waitcnt lgkmcnt(6)\n"
      "v_mfma_f32_16x16x32_bf16 v[80:83], v[140:143], v[164:167], v[80:83]\n"
      "ds_read_b64_tr_b16 v[146:147], v2 offset:51712\n"
      "v_mfma_f32_16x16x32_bf16 v[84:87], v[136:139], v[168:171], v[84:87]\n"
      "ds_read_b128 v[160:163], v194 offset:0\n"
      "s_waitcnt lgkmcnt(6)\n"
      "v_mfma_f32_16x16x32_bf16 v[84:87], v[140:143], v[172:175], v[84:87]\n"
      "ds_read_b64_tr_b16 v[148:149], v2 offset:59392\n"
      "v_mfma_f32_16x16x32_bf16 v[68:71], v[128:131], v[168:171], v[68:71]\n"
      "ds_read_b64_tr_b16 v[150:151], v2 offset:59904\n"
      "v_mfma_f32_16x16x32_bf16 v[68:71], v[132:135], v[172:175], v[68:71]\n"
      "ds_read_b128 v[164:167], v195 offset:0\n"
      "v_mfma_f32_16x16x32_bf16 v[72:75], v[128:131], v[176:179], v[72:75]\n"
      "ds_read_b64_tr_b16 v[152:153], v3 offset:51200\n"
      "s_waitcnt lgkmcnt(6)\n"
      "v_mfma_f32_16x16x32_bf16 v[72:75], v[132:135], v[180:183], v[72:75]\n"
      "ds_read_b64_tr_b16 v[154:155], v3 offset:51712\n"
      "v_mfma_f32_16x16x32_bf16 v[88:91], v[136:139], v[176:179], v[88:91]\n"
      "ds_read_b64_tr_b16 v[156:157], v3 offset:59392\n"
      "v_mfma_f32_16x16x32_bf16 v[88:91], v[140:143], v[180:183], v[88:91]\n"
      "ds_read_b64_tr_b16 v[158:159], v3 offset:59904\n"
      "v_mfma_f32_16x16x32_bf16 v[92:95], v[136:139], v[184:187], v[92:95]\n"
      "ds_read_b128 v[168:171], v194 offset:2048\n"
      "v_mfma_f32_16x16x32_bf16 v[92:95], v[140:143], v[188:191], v[92:95]\n"
      "ds_read_b128 v[172:175], v195 offset:2048\n"
      "v_mfma_f32_16x16x32_bf16 v[76:79], v[128:131], v[184:187], v[76:79]\n"
      "ds_read_b128 v[176:179], v194 offset:4096\n"
      "v_mfma_f32_16x16x32_bf16 v[76:79], v[132:135], v[188:191], v[76:79]\n"
      "ds_read_b128 v[180:183], v195 offset:4096\n"
      "s_waitcnt lgkmcnt(6)\n"
      "v_mfma_f32_16x16x32_bf16 v[64:67], v[144:147], v[160:163], v[64:67]\n"
      "ds_read_b128 v[184:187], v194 offset:6144\n"
      "v_mfma_f32_16x16x32_bf16 v[64:67], v[148:151], v[164:167], v[64:67]\n"
      "ds_read_b128 v[188:191], v195 offset:6144\n"
      "v_mfma_f32_16x16x32_bf16 v[80:83], v[152:155], v[160:163], v[80:83]\n"
      "s_waitcnt lgkmcnt(5)\n"
      "v_mfma_f32_16x16x32_bf16 v[80:83], v[156:159], v[164:167], v[80:83]\n"
      "v_mfma_f32_16x16x32_bf16 v[84:87], v[152:155], v[168:171], v[84:87]\n"
      "s_waitcnt lgkmcnt(3)\n"
      "v_mfma_f32_16x16x32_bf16 v[84:87], v[156:159], v[172:175], v[84:87]\n"
      "v_mfma_f32_16x16x32_bf16 v[68:71], v[144:147], v[168:171], v[68:71]\n"
      "v_mfma_f32_16x16x32_bf16 v[68:71], v[148:151], v[172:175], v[68:71]\n"
      "v_mfma_f32_16x16x32_bf16 v[72:75], v[144:147], v[176:179], v[72:75]\n"
      "s_waitcnt lgkmcnt(0)\n"
      "v_mfma_f32_16x16x32_bf16 v[72:75], v[148:151], v[180:183], v[72:75]\n"
      "s_waitcnt vmcnt(0)\n"
      "v_xad_u32 v199, v96, s92, v19\n"
      "v_xad_u32 v199, v97, s93, v199\n"
      "v_mfma_f32_16x16x32_bf16 v[88:91], v[152:155], v[176:179], v[88:91]\n"
      "v_xad_u32 v199, v98, s94, v199\n"
      "v_xad_u32 v199, v99, s95, v199\n"
      "v_mfma_f32_16x16x32_bf16 v[88:91], v[156:159], v[180:183], v[88:91]\n"
      "v_xad_u32 v199, v100, s92, v199\n"
      "v_xad_u32 v199, v101, s93, v199\n"
      "v_mfma_f32_16x16x32_bf16 v[92:95], v[152:155], v[184:187], v[92:95]\n"
      "v_xad_u32 v199, v102, s94, v199\n"
      "v_xad_u32 v199, v103, s95, v199\n"
      "v_mfma_f32_16x16x32_bf16 v[92:95], v[156:159], v[188:191], v[92:95]\n"
      "v_xad_u32 v199, v104, s92, v199\n"
      "v_xad_u32 v199, v105, s93, v199\n"
      "v_mfma_f32_16x16x32_bf16 v[76:79], v[144:147], v[184:187], v[76:79]\n"
      "v_xad_u32 v199, v106, s94, v199\n"
      "v_xad_u32 v199, v107, s95, v199\n"
      "v_mfma_f32_16x16x32_bf16 v[76:79], v[148:151], v[188:191], v[76:79]\n"
      "v_xad_u32 v199, v108, s92, v199\n"
      "v_xad_u32 v199, v109, s93, v199\n"
      "v_xad_u32 v199, v110, s94, v199\n"
      "v_xad_u32 v199, v111, s95, v199\n"
      "L10_%=:\n"
      "v_cmp_ne_u64 s[90:91], v[114:115], v[18:19]\n"
      "s_nop 1\n"
      "v_cmp_ne_u32 vcc, v199, v112\n"
      "s_nop 0\n"
      "s_or_b64 vcc, vcc, s[90:91]\n"
      "s_cbranch_scc1 L8_%=\n"
      "L12_%=:\n"
      "v_mfma_f32_16x16x32_bf16 v[64:67], v[10:13], v[96:99], v[64:67]\n"
      "v_mfma_f32_16x16x32_bf16 v[68:71], v[8:11], v[96:99], v[68:71]\n"
      "v_mfma_f32_16x16x32_bf16 v[80:83], v[10:13], v[104:107], v[80:83]\n"
      "ds_read_b32 v29, v6 offset:32768\n"
      "s_nop 3\n"
      "v_mfma_f32_16x16x32_bf16 v[84:87], v[8:11], v[104:107], v[84:87]\n"
      "v_cvt_pk_bf16_f32 v200, v64, v65\n"
      "v_cvt_pk_bf16_f32 v201, v66, v67\n"
      "v_pk_max_i16 v200, v200, 0\n"
      "v_mfma_f32_16x16x32_bf16 v[72:75], v[10:13], v[100:103], v[72:75]\n"
      "v_pk_max_i16 v201, v201, 0\n"
      "v_cvt_pk_bf16_f32 v204, v68, v69\n"
      "v_cvt_pk_bf16_f32 v205, v70, v71\n"
      "v_mfma_f32_16x16x32_bf16 v[88:91], v[10:13], v[108:111], v[88:91]\n"
      "v_pk_max_i16 v204, v204, 0\n"
      "v_pk_max_i16 v205, v205, 0\n"
      "v_cvt_pk_bf16_f32 v202, v80, v81\n"
      "v_mfma_f32_16x16x32_bf16 v[76:79], v[8:11], v[100:103], v[76:79]\n"
      "v_cvt_pk_bf16_f32 v203, v82, v83\n"
      "v_pk_max_i16 v202, v202, 0\n"
      "v_pk_max_i16 v203, v203, 0\n"
      "v_mfma_f32_16x16x32_bf16 v[92:95], v[8:11], v[108:111], v[92:95]\n"
      "v_cvt_pk_bf16_f32 v206, v84, v85\n"
      "v_cvt_pk_bf16_f32 v207, v86, v87\n"
      "v_pk_max_i16 v206, v206, 0\n"
      "v_mfma_f32_16x16x32_bf16 v[128:131], v[14:17], v[200:203], 0\n"
      "v_pk_max_i16 v207, v207, 0\n"
      "v_cvt_pk_bf16_f32 v208, v72, v73\n"
      "v_cvt_pk_bf16_f32 v209, v74, v75\n"
      "v_mfma_f32_16x16x32_bf16 v[132:135], v[14:17], v[204:207], 0\n"
      "v_pk_max_i16 v208, v208, 0\n"
      "v_pk_max_i16 v209, v209, 0\n"
      "v_cvt_pk_bf16_f32 v210, v88, v89\n"
      "v_cvt_pk_bf16_f32 v211, v90, v91\n"
      "v_pk_max_i16 v210, v210, 0\n"
      "v_pk_max_i16 v211, v211, 0\n"
      "v_cvt_pk_bf16_f32 v212, v76, v77\n"
      "v_cvt_pk_bf16_f32 v213, v78, v79\n"
      "v_mfma_f32_16x16x32_bf16 v[136:139], v[14:17], v[208:211], 0\n"
      "v_pk_max_i16 v212, v212, 0\n"
      "v_pk_max_i16 v213, v213, 0\n"
      "v_cvt_pk_bf16_f32 v214, v92, v93\n"
      "v_cvt_pk_bf16_f32 v215, v94, v95\n"
      "v_pk_max_i16 v214, v214, 0\n"
      "v_pk_max_i16 v215, v215, 0\n"
      "v_mov_b32_dpp v128, v132 quad_perm:[0,1,2,3] row_mask:2 bank_mask:0xf\n"
      "s_nop 0\n"
      "v_mfma_f32_16x16x32_bf16 v[140:143], v[14:17], v[212:215], 0\n"
      "v_mov_b32_dpp v128, v136 quad_perm:[0,1,2,3] row_mask:4 bank_mask:0xf\n"
      "v_mov_b32 v28, s24\n"
      "s_nop 5\n"
      "v_mov_b32_dpp v128, v140 quad_perm:[0,1,2,3] row_mask:8 bank_mask:0xf\n"
      "v_cvt_f64_f32 v[118:119], v128\n"
      "v_cmp_nlt_f32 vcc, |v128|, s50\n"
      "v_add_f64 v[116:117], v[118:119], s[86:87]\n"
      "v_add_u32 v117, 0xce500000, v117\n"
      "s_cbranch_vccnz L7_%=\n"
      "buffer_atomic_add_x2 v[116:117], v198, s[28:31], s15 offen sc0\n"
      "v_add_f64 v[118:119], v[118:119], s[86:87]\n"
      "v_add_u32 v119, 0x10000000, v119\n"
      "L11_%=:\n"
      "s_brev_b32 s7, 8\n"
      "s_movk_i32 s34, 0xfff\n"
      "s_waitcnt vmcnt(0) lgkmcnt(0)\n"
      "v_lshl_add_u64 v[118:119], v[116:117], 0, v[118:119]\n"
      "v_add_f64 v[116:117], v[118:119], s[88:89]\n"
      "v_subrev_u32 v25, 0x41b00000, v119\n"
      "v_cvt_f32_f64 v24, v[116:117]\n"
      "v_cmpx_gt_u32 vcc, s7, v25\n"
      "v_cmp_lt_u32 vcc, s34, v25\n"
      "s_cbranch_execz L20_%=\n"
      "s_cbranch_vccnz L17_%=\n"
      "L19_%=:\n"
      "v_cvt_pk_bf16_f32 v24, 0, v24\n"
      "v_add_f32 v24, s27, v24\n"
      "v_cvt_pk_bf16_f32 v24, 0, v24\n"
      "v_add_f32 v24, v29, v24\n"
      "v_cvt_pk_bf16_f32 v24, 0, v24\n"
      "v_mul_f32 v24, 0xbfb8aa3b, v24\n"
      "v_exp_f32 v24, v24\n"
      "s_nop 0\n"
      "v_add_f32 v24, 1.0, v24\n"
      "v_rcp_f32 v24, v24\n"
      "s_nop 0\n"
      "v_cvt_pk_bf16_f32 v24, v24, v24\n"
      "global_store_short v7, v24, s[48:49] \n"
      "L20_%=:\n"
      "s_or_b32 s7, s4, s35\n"
      "s_cbranch_scc1 L21_%=\n"
      "s_mov_b64 exec, 1\n"
      "buffer_store_dword v28, off, s[28:31], s6 sc1\n"
      "L21_%=:\n"
      "s_endpgm\n"
      "L8_%=:\n"
      "s_sub_u32 s10, s10, 1\n"
      "s_cbranch_scc1 L12_%=\n"
      "buffer_load_dwordx4 v[96:99], v1, s[28:31], s13 offen offset:0 sc1\n"
      "buffer_load_dwordx4 v[100:103], v1, s[28:31], s13 offen offset:1024 sc1\n"
      "buffer_load_dwordx4 v[104:107], v1, s[28:31], s13 offen offset:2048 sc1\n"
      "buffer_load_dwordx4 v[108:111], v1, s[28:31], s13 offen offset:3072 sc1\n"
      "buffer_load_dword v112, v196, s[28:31], s13 offen sc1\n"
      "buffer_load_dwordx2 v[114:115], v197, s[28:31], s84 offen sc1\n"
      "s_waitcnt vmcnt(0)\n"
      "v_xad_u32 v199, v96, s92, v19\n"
      "v_xad_u32 v199, v97, s93, v199\n"
      "v_xad_u32 v199, v98, s94, v199\n"
      "v_xad_u32 v199, v99, s95, v199\n"
      "v_xad_u32 v199, v100, s92, v199\n"
      "v_xad_u32 v199, v101, s93, v199\n"
      "v_xad_u32 v199, v102, s94, v199\n"
      "v_xad_u32 v199, v103, s95, v199\n"
      "v_xad_u32 v199, v104, s92, v199\n"
      "v_xad_u32 v199, v105, s93, v199\n"
      "v_xad_u32 v199, v106, s94, v199\n"
      "v_xad_u32 v199, v107, s95, v199\n"
      "v_xad_u32 v199, v108, s92, v199\n"
      "v_xad_u32 v199, v109, s93, v199\n"
      "v_xad_u32 v199, v110, s94, v199\n"
      "v_xad_u32 v199, v111, s95, v199\n"
      "s_branch L10_%=\n"
      "L7_%=:\n"
      "s_lshl_b32 s7, s3, 13\n"
      "s_and_b32 s34, s35, 0x80\n"
      "s_lshl1_add_u32 s7, s34, s7\n"
      "s_add_u32 s7, s7, 5304320\n"
      "s_lshl3_add_u32 s7, s5, s7\n"
      "s_lshr_b32 s34, s5, 6\n"
      "s_lshl_b32 s34, 0x1000, s34\n"
      "s_add_u32 s34, s34, 0x10000080\n"
      "s_mov_b64 exec, vcc\n"
      "buffer_store_dword v128, v196, s[28:31], s7 offen sc1\n"
      "v_mov_b32 v116, 0\n"
      "v_mov_b32 v117, s34\n"
      "s_waitcnt vmcnt(0)\n"
      "s_mov_b64 exec, -1\n"
      "v_mov_b32 v118, v116\n"
      "v_add_u32 v119, 0x41b00000, v117\n"
      "buffer_atomic_add_x2 v[116:117], v198, s[28:31], s15 offen sc0\n"
      "s_branch L11_%=\n"
      "L17_%=:\n"
      "buffer_inv sc1\n"
      "s_lshl_b32 s7, s3, 13\n"
      "s_and_b32 s34, s35, 0x80\n"
      "s_lshl1_add_u32 s7, s34, s7\n"
      "s_add_u32 s7, s7, 5304320\n"
      "s_movk_i32 s34, 0x1000\n"
      "v_and_b32 v119, 0xfff, v25\n"
      "v_or_b32 v119, 0x41b00000, v119\n"
      "v_add_f64 v[116:117], v[118:119], s[88:89]\n"
      "v_cvt_f32_f64 v24, v[116:117]\n"
      "L18_%=:\n"
      "buffer_load_dword v26, v196, s[28:31], s7 offen sc1\n"
      "v_and_b32 v116, s34, v25\n"
      "v_cmp_ne_u32 vcc, 0, v116\n"
      "s_waitcnt vmcnt(0)\n"
      "s_nop 0\n"
      "v_cndmask_b32 v26, 0, v26, vcc\n"
      "v_add_f32 v24, v24, v26\n"
      "s_add_u32 s7, s7, 512\n"
      "s_lshl_b32 s34, s34, 1\n"
      "s_bitcmp0_b32 s34, 28\n"
      "s_cbranch_scc1 L18_%=\n"
      "s_branch L19_%=\n"
      :
      : [kp] "s"(__builtin_amdgcn_kernarg_segment_ptr()), [bid] "s"(blockIdx.x), [tid] "v"(threadIdx.x)
      : "memory", "scc", "vcc", "v1", "v2", "v3", "v4", "v5", "v6", "v7", "v8", "v9", "v10", "v11", "v12", "v13", "v14", "v15", "v16", "v17", "v18", "v19", "v20", "v21", "v22", "v23", "v24", "v25", "v26", "v27", "v28", "v29", "v30", "v31", "v32", "v33", "v34", "v35", "v36", "v37", "v38", "v39", "v40", "v41", "v42", "v43", "v44", "v45", "v46", "v47", "v48", "v49", "v50", "v51", "v52", "v53", "v54", "v55", "v56", "v57", "v58", "v59", "v60", "v61", "v62", "v63", "v64", "v65", "v66", "v67", "v68", "v69", "v70", "v71", "v72", "v73", "v74", "v75", "v76", "v77", "v78", "v79", "v80", "v81", "v82", "v83", "v84", "v85", "v86", "v87", "v88", "v89", "v90", "v91", "v92", "v93", "v94", "v95", "v96", "v97", "v98", "v99", "v100", "v101", "v102", "v103", "v104", "v105", "v106", "v107", "v108", "v109", "v110", "v111", "v112", "v113", "v114", "v115", "v116", "v117", "v118", "v119", "v120", "v121", "v122", "v123", "v124", "v125", "v126", "v127", "v128", "v129", "v130", "v131", "v132", "v133", "v134", "v135", "v136", "v137", "v138", "v139", "v140", "v141", "v142", "v143", "v144", "v145", "v146", "v147", "v148", "v149", "v150", "v151", "v152", "v153", "v154", "v155", "v156", "v157", "v158", "v159", "v160", "v161", "v162", "v163", "v164", "v165", "v166", "v167", "v168", "v169", "v170", "v171", "v172", "v173", "v174", "v175", "v176", "v177", "v178", "v179", "v180", "v181", "v182", "v183", "v184", "v185", "v186", "v187", "v188", "v189", "v190", "v191", "v192", "v193", "v194", "v195", "v196", "v197", "v198", "v199", "v200", "v201", "v202", "v203", "v204", "v205", "v206", "v207", "v208", "v209", "v210", "v211", "v212", "v213", "v214", "v215", "s3", "s4", "s5", "s6", "s7", "s8", "s9", "s10", "s11", "s12", "s13", "s14", "s15", "s16", "s17", "s18", "s19", "s20", "s21", "s22", "s23", "s24", "s25", "s26", "s27", "s28", "s29", "s30", "s31", "s33", "s34", "s35", "s36", "s37", "s38", "s39", "s40", "s41", "s42", "s43", "s44", "s45", "s46", "s47", "s48", "s49", "s50", "s51", "s52", "s53", "s54", "s55", "s56", "s57", "s58", "s59", "s60", "s61", "s62", "s63", "s64", "s65", "s66", "s67", "s68", "s69", "s70", "s71", "s72", "s73", "s74", "s75", "s76", "s77", "s78", "s79", "s80", "s81", "s82", "s83", "s84", "s85", "s86", "s87", "s88", "s89", "s90", "s91", "s92", "s93", "s94", "s95");
}

static void schedule(unsigned char (*s)[NS]) {
  static const char* const S[32] = {"0145236789abcdefijghklopmn", "32014567ab89efcdghijklmnop", "2301674589abcdghefijmnklop", "1023458967cdabefghklijmnop",
    "10234567ab89efcdghijklopmn", "0123458967abcdefijghklmnop", "3245016789abcdghefklijmnop", "2301674589cdabefghijmnklop",
    "2301674589abcdefghklijopmn", "1023458967abcdghefijklmnop", "0145236789cdabefijghklmnop", "32014567ab89efcdghijmnklop",
    "3245016789cdabefghijmnklop", "23014567ab89cdefijghklmnop", "1023674589abefcdghijklopmn", "0123458967abcdghefklijmnop",
    "4501672389abefcdijghklmnop", "76450123ab89cdefghijmnklop", "6745018923cdabghefijklmnop", "5467230189abcdefghklijopmn",
    "5467018923cdabefghijklopmn", "45670123ab89cdefghklijmnop", "7601452389abcdghefijmnklop", "6745230189abefcdijghklmnop",
    "6701452389abcdghefijklopmn", "5467018923cdabefghijklmnop", "45672301ab89cdefghklijmnop", "7645012389abefcdijghmnklop",
    "7601452389cdabefghklijmnop", "6745018923abcdefghijklopmn", "5467012389abefcdijghmnklop", "45672301ab89cdghefijklmnop"};
  for (int ty = 0; ty < 32; ++ty) {
    const char* p = S[ty];
    for (int k = 0; k < NS; ++k) s[ty][k] = (unsigned char)(p[k] <= '9' ? p[k] - '0' : p[k] - 'a' + 10);
  }
}

extern "C" void kernel_launch(void* const* d_in, const int* in_sizes, int n_in, void* d_out, int out_size, void* d_ws,
                              size_t ws_size, hipStream_t stream) {
  Args a;
  a.idx = d_in[0]; a.idxb = Bsz * F * 4; a.W1 = d_in[5]; a.w1b = F * D * H * 2 + 4096; a.table = d_in[1]; a.tabb = (unsigned)in_sizes[1] * 2u + 4096u;
  a.ws = d_ws; a.wsb = WS_END; a.f0 = a.f1 = a.f2 = a.f3 = 0x00020000;
  a.b1 = d_in[6]; a.W2 = d_in[7]; a.xw = d_in[2]; a.ww = d_in[3]; a.bw = d_in[4]; a.b2 = d_in[8]; a.out = d_out; a.pad = 0;
  unsigned char s[32][NS];
  schedule(s);
  for (int ty = 0; ty < 32; ++ty) {
    Row& r = a.row[ty];
    r.wide = 0; r.last = 0; r.pad[0] = r.pad[1] = 0;
    for (int k = 0; k < 56; ++k) r.u[k] = k < NS ? s[ty][k] : 0;
    for (int k = 0; k < NS; ++k) if (s[ty][k] < NS1) { r.wide |= 1u << k; r.last = (unsigned char)k; }
    r.u2 = s[ty][2];
  }
  (void)hipFuncSetAttribute((const void*)k1, hipFuncAttributeMaxDynamicSharedMemorySize, LDS_BYTES);
  k1<<<256, 512, LDS_BYTES, stream>>>(a);
}
```
